```python
import torch

def _softmax(x, axis=-1):
    return torch.softmax(x, dim=axis)
def _gelu(x):
    return torch.nn.functional.gelu(x, approximate="tanh")
def _silu(x):
    return torch.nn.functional.silu(x)
def _sigmoid(x):
    return torch.sigmoid(x)
def _layernorm(x, axis=-1, eps=1e-5):
    m = x.mean(dim=axis, keepdim=True)
    v = x.var(dim=axis, keepdim=True, unbiased=False)
    return (x - m) / torch.sqrt(v + eps)
def _scatter_add(tgt, idx, src, axis=0):
    return tgt.clone().index_add_(axis, idx, src)
def _segment_sum(src, seg, n):
    out = torch.zeros((n,) + src.shape[1:], dtype=src.dtype)
    return out.index_add_(0, seg, src)
def _pad(x, pw):
    flat = tuple(v for pair in reversed(pw) for v in pair)
    return torch.nn.functional.pad(x, flat)

kernel_name = 'standalone__deep_cross_wide_deep__recsys'

def setup_inputs():

    _w = (lambda a: (lambda t: t.to(torch.bfloat16) if t.is_floating_point() else t)(torch.as_tensor(a)))
    B, F, D = 1024, 100, 128
    V, Nd = min(1000, 100000), 13
    Din = F * D
    H = 4 * D
    return {
        "idx": _w((lambda lo, hi, *s: torch.randint(lo, hi, s))(0, V, B, F).to(torch.int64)),
        "table": _w((lambda *s: torch.randn(*s, dtype=torch.bfloat16) * 1.6)(V, D) * 0.05),
        "x_wide": _w((lambda *s: torch.randn(*s, dtype=torch.bfloat16) * 1.6)(B, Nd)),
        "w_wide": _w((lambda *s: torch.randn(*s, dtype=torch.bfloat16) * 1.6)(Nd) * 0.05),
        "b_wide": _w((lambda *s: torch.randn(*s, dtype=torch.bfloat16) * 1.6)(1) * 0.01),
        "W1": _w((lambda *s: torch.randn(*s, dtype=torch.bfloat16) * 1.6)(Din, H) * (2.0 / Din) ** 0.5),
        "b1": _w((lambda *s: torch.randn(*s, dtype=torch.bfloat16) * 1.6)(H) * 0.01),
        "W2": _w((lambda *s: torch.randn(*s, dtype=torch.bfloat16) * 1.6)(H) * (1.0 / H) ** 0.5),
        "b2": _w((lambda *s: torch.randn(*s, dtype=torch.bfloat16) * 1.6)(1) * 0.01),
    }

def reference(idx, table, x_wide, w_wide, b_wide, W1, b1, W2, b2):
    emb = table[idx]
    deep_in = emb.reshape(emb.shape[0], -1)
    h = deep_in @ W1 + b1
    h = h * (lambda x, dt: x.to(dt))(h > 0, h.dtype)
    deep_logit = h @ W2 + b2
    wide_logit = x_wide @ w_wide + b_wide
    return _sigmoid(wide_logit + deep_logit)
```

```cpp
#include <hip/hip_runtime.h>
#include <hip/hip_bf16.h>

typedef short    bf16x8 __attribute__((ext_vector_type(8)));
typedef float    f32x16 __attribute__((ext_vector_type(16)));
typedef float    f32x4  __attribute__((ext_vector_type(4)));
typedef unsigned u32x4  __attribute__((ext_vector_type(4)));
typedef unsigned u32x2  __attribute__((ext_vector_type(2)));
#define LAS __attribute__((address_space(3)))
#define SB() __builtin_amdgcn_sched_barrier(0)

constexpr int NB_ = 1024, NF = 100, ND = 128, NV = 1000, NW = 13, NH = 512;
constexpr int TPITCH   = 80;
constexpr int TBL_LDS  = 80 * 1024;
constexpr int IDX_WAVE = 14 * 1024;
constexpr int LDS_BYTES = TBL_LDS + 4 * IDX_WAVE;
constexpr int SRD3 = 0x00020000;

__device__ __forceinline__ float rbf(float x) { return (float)(__bf16)x; }
__device__ __forceinline__ float bf2f(unsigned short h) { return __uint_as_float((unsigned)h << 16); }

__device__ __forceinline__ void dma16(__amdgpu_buffer_rsrc_t rs, unsigned ldsoff, int voff, int soff) {
  __builtin_amdgcn_raw_ptr_buffer_load_lds(rs, (LAS void*)(size_t)ldsoff, 16, voff, soff, 0, 0);
}
__device__ __forceinline__ u32x4 lds128(unsigned a) { return *(const LAS u32x4*)(size_t)a; }

struct St {
  f32x16 acc[4][2];
  u32x4  A[2][4][2];
  u32x4  B[2][2][2];
  u32x2  W[4][8];
  u32x4  I[2][4];
};

template <int CUR> __device__ __forceinline__ void mf(St& c, int k) {
  const int s = k >> 3, mb = (k >> 1) & 3, nb = k & 1;
  c.acc[mb][nb] = __builtin_amdgcn_mfma_f32_32x32x16_bf16(__builtin_bit_cast(bf16x8, c.A[CUR][mb][s]),
                                                          __builtin_bit_cast(bf16x8, c.B[CUR][s][nb]), c.acc[mb][nb], 0, 0, 0);
}
template <int NX, int SL> __device__ __forceinline__ void swp(St& c, int j) {
  auto r = __builtin_amdgcn_permlane16_swap(c.W[SL][j][0], c.W[SL][j][1], false, false);
  c.W[SL][j][0] = r[0]; c.W[SL][j][1] = r[1];
}
template <int NX, int SL> __device__ __forceinline__ void prm(St& c, int i, int s) {
  c.B[NX][s][0][i] = __builtin_amdgcn_perm(c.W[SL][2 * i + 1][s], c.W[SL][2 * i][s], 0x05040100u);
  c.B[NX][s][1][i] = __builtin_amdgcn_perm(c.W[SL][2 * i + 1][s], c.W[SL][2 * i][s], 0x07060302u);
}
template <int SL> __device__ __forceinline__ void wld(St& c, int j, __amdgpu_buffer_rsrc_t rs, int voff, int soff) {
  c.W[SL][j] = __builtin_amdgcn_raw_buffer_load_b64(rs, voff + (j & 3) * 1024, soff + (j >> 2) * 4096, 0);
}

template <int U, bool MF, bool PF>
__device__ __forceinline__ void step(St& c, unsigned g16, unsigned iaddr, __amdgpu_buffer_rsrc_t rsW, int wv, int soff) {
  constexpr int CUR = U & 1, NX = CUR ^ 1, SL = (U + 1) & 3;
  constexpr int IB = (U == 3) ? 1 : 0;
  constexpr int IC = (U + 1) & 3;
  unsigned ad[4];
#pragma unroll
  for (int mb = 0; mb < 4; ++mb) {
    if (MF) mf<CUR>(c, mb);
    if (PF) {
      ad[mb] = __umul24(c.I[IB][mb][IC], TPITCH) + g16;
      c.A[NX][mb][0] = lds128(ad[mb]);
      c.A[NX][mb][1] = lds128(ad[mb] + 32);
    }
    SB();
  }
#pragma unroll
  for (int i = 0; i < 4; ++i) {
    if (MF) mf<CUR>(c, 4 + 3 * i);
    if (PF) { swp<NX, SL>(c, 2 * i); swp<NX, SL>(c, 2 * i + 1);
              if (U == 0) c.I[1][i] = lds128(iaddr + i * 3584); }
    SB();
    if (MF) mf<CUR>(c, 5 + 3 * i);
    if (PF) { prm<NX, SL>(c, i, 0); prm<NX, SL>(c, i, 1); }
    SB();
    if (MF) mf<CUR>(c, 6 + 3 * i);
    if (PF) { wld<SL>(c, 2 * i, rsW, wv, soff); wld<SL>(c, 2 * i + 1, rsW, wv, soff); }
    SB();
  }
}

__global__ void __launch_bounds__(256, 1)
gemm_kernel(const int* __restrict__ idx, const unsigned short* __restrict__ table,
            const unsigned short* __restrict__ W1, float* __restrict__ slab) {
  extern __shared__ __attribute__((aligned(16))) char smem[];
  const unsigned lbase = (unsigned)(size_t)(LAS char*)smem;
  const int tid = threadIdx.x, lane = tid & 63;
  const int w = __builtin_amdgcn_readfirstlane(tid >> 6);
  const int bid = blockIdx.x;
  const int xcd = bid & 7, jj = bid >> 3;
  const int q = xcd >> 1, nt = 4 * (xcd & 1) + (jj & 3), mt = jj >> 2;

  const __amdgpu_buffer_rsrc_t rsW = __builtin_amdgcn_make_buffer_rsrc(
      (void*)(W1 + ((size_t)(25 * w * ND + 32 * q) * NH + nt * 64)), (short)0, (24 * ND + 32) * 1024 - 896, SRD3);
  const __amdgpu_buffer_rsrc_t rsT = __builtin_amdgcn_make_buffer_rsrc((void*)table, (short)0, NV * ND * 2, SRD3);
  const __amdgpu_buffer_rsrc_t rsI = __builtin_amdgcn_make_buffer_rsrc((void*)(idx + mt * 128 * NF), (short)0, 128 * NF * 4, SRD3);

  St c;
  const int c16 = lane & 15, h = (lane >> 4) & 1, g = lane >> 5, r = lane & 31;
  const int wv = (8 * (2 * h + g)) * 1024 + c16 * 8;

#pragma unroll
  for (int j = 0; j < 8; ++j) wld<0>(c, j, rsW, wv, 0);
#pragma unroll
  for (int u = 0; u < 5; ++u) {
    const int s = u * 64 + lane, row = s / 5, ch = s - row * 5;
    const int v = (ch < 4) ? row * 256 + ch * 16 : 0x40000000;
#pragma unroll
    for (int k = 0; k < 4; ++k)
      dma16(rsT, lbase + ((w * 4 + k) * 5 + u) * 1024, v, (w * 4 + k) * 16384 + q * 64);
  }
#pragma unroll
  for (int u = 0; u < 7; ++u) {
    const int s = u * 64 + lane, lr = s / 7, p = s - lr * 7;
    const int v = (lr ^ (32 * (w & 1))) * 400 + p * 16;
#pragma unroll
    for (int hf = 0; hf < 2; ++hf)
      dma16(rsI, lbase + TBL_LDS + w * IDX_WAVE + (hf * 7 + u) * 1024, v, (hf ^ (w >> 1)) * 25600 + 100 * w);
  }
#pragma unroll
  for (int j = 0; j < 8; ++j) wld<1>(c, j, rsW, wv, 1 * 131072);
#pragma unroll
  for (int j = 0; j < 8; ++j) wld<2>(c, j, rsW, wv, 2 * 131072);
#pragma unroll
  for (int j = 0; j < 8; ++j) wld<3>(c, j, rsW, wv, 3 * 131072);
#pragma unroll
  for (int mb = 0; mb < 4; ++mb)
#pragma unroll
    for (int nb = 0; nb < 2; ++nb)
#pragma unroll
      for (int e = 0; e < 16; ++e) c.acc[mb][nb][e] = 0.f;
  asm volatile("s_waitcnt vmcnt(24)" ::: "memory");
  __builtin_amdgcn_s_barrier();
  asm volatile("" ::: "memory");

  const unsigned g16 = lbase + g * 16;
  unsigned iaddr = lbase + TBL_LDS + w * IDX_WAVE + r * 112;
#pragma unroll
  for (int mb = 0; mb < 4; ++mb) c.I[1][mb] = lds128(iaddr + mb * 3584);
  iaddr += 16;
  SB();
  step<3, false, true>(c, g16, iaddr, rsW, wv, 4 * 131072);

  int soff = 5 * 131072;
#pragma unroll 1
  for (int p = 0; p < 6; ++p) {
#pragma unroll
    for (int mb = 0; mb < 4; ++mb) c.I[0][mb] = c.I[1][mb];
    step<0, true, true>(c, g16, iaddr, rsW, wv, soff);
    step<1, true, true>(c, g16, iaddr, rsW, wv, soff + 131072);
    step<2, true, true>(c, g16, iaddr, rsW, wv, soff + 2 * 131072);
    step<3, true, true>(c, g16, iaddr, rsW, wv, soff + 3 * 131072);
    soff += 4 * 131072;
    iaddr += 16;
  }
  step<0, true, false>(c, g16, iaddr, rsW, wv, soff);

  __syncthreads();
#pragma unroll
  for (int i = 1; i < 4; ++i) {
    const unsigned d = lbase + (((w ^ i) * 3 + (i - 1)) * 8192) + lane * 16;
#pragma unroll
    for (int nb = 0; nb < 2; ++nb)
#pragma unroll
      for (int qd = 0; qd < 4; ++qd) {
        f32x4 v = {c.acc[i][nb][4 * qd], c.acc[i][nb][4 * qd + 1], c.acc[i][nb][4 * qd + 2], c.acc[i][nb][4 * qd + 3]};
        *(LAS f32x4*)(size_t)(d + (nb * 4 + qd) * 1024) = v;
      }
  }
  __syncthreads();
  const __amdgpu_buffer_rsrc_t rsS = __builtin_amdgcn_make_buffer_rsrc(
      (void*)((char*)slab + (size_t)(mt * 4 + w) * 8 * 32768 + q * 8192 + nt * 1024), (short)0, 8 * 32768, SRD3);
  const unsigned sa = lbase + w * 3 * 8192 + lane * 16;
  const int sv = g * 32768 + r * 16;
#pragma unroll
  for (int nb = 0; nb < 2; ++nb)
#pragma unroll
    for (int qd = 0; qd < 4; ++qd) {
      f32x4 v = {c.acc[0][nb][4 * qd], c.acc[0][nb][4 * qd + 1], c.acc[0][nb][4 * qd + 2], c.acc[0][nb][4 * qd + 3]};
#pragma unroll
      for (int i = 0; i < 3; ++i) v += *(const LAS f32x4*)(size_t)(sa + i * 8192 + (nb * 4 + qd) * 1024);
      __builtin_amdgcn_raw_buffer_store_b128(__builtin_bit_cast(u32x4, v), rsS, sv + qd * 65536 + nb * 512, 0, 0);
    }
}

__global__ void __launch_bounds__(256)
finish_kernel(const float* __restrict__ slab, const unsigned short* __restrict__ xw, const unsigned short* __restrict__ ww,
              const unsigned short* __restrict__ bw, const unsigned short* __restrict__ b1, const unsigned short* __restrict__ W2,
              const unsigned short* __restrict__ b2, __bf16* __restrict__ out) {
  __shared__ f32x4 red[4];
  const int blk = blockIdx.x, tid = threadIdx.x, lane = tid & 63, w = tid >> 6;
  const f32x4* sp = (const f32x4*)slab + (size_t)blk * 2048;
  f32x4 v[2][4];
#pragma unroll
  for (int i = 0; i < 2; ++i)
#pragma unroll
    for (int q = 0; q < 4; ++q) v[i][q] = sp[q * 512 + i * 256 + tid];
  float wide = 0.f;
  if (tid < 4) {
    const unsigned short* x = xw + (blk * 4 + tid) * NW;
#pragma unroll
    for (int k = 0; k < NW; ++k) wide += bf2f(x[k]) * bf2f(ww[k]);
    wide = rbf(rbf(wide) + bf2f(bw[0]));
  }
  f32x4 a = {0.f, 0.f, 0.f, 0.f};
#pragma unroll
  for (int i = 0; i < 2; ++i) {
    const int e = i * 256 + tid, cc = e & 31;
    const int n = (e >> 6) * 64 + 4 * (cc & 15) + 2 * (cc >> 4) + ((e >> 5) & 1);
    const float bb = bf2f(b1[n]), w2 = bf2f(W2[n]);
    const f32x4 s = (v[i][0] + v[i][1]) + (v[i][2] + v[i][3]);
#pragma unroll
    for (int k = 0; k < 4; ++k) {
      float hh = rbf(rbf(s[k]) + bb);
      hh = hh > 0.f ? hh : hh * 0.f;
      a[k] += hh * w2;
    }
  }
#pragma unroll
  for (int o = 32; o >= 1; o >>= 1)
#pragma unroll
    for (int k = 0; k < 4; ++k) a[k] += __shfl_xor(a[k], o, 64);
  if (lane == 0) red[w] = a;
  __syncthreads();
  if (tid < 4) {
    const float* rp = (const float*)red;
    float d = (rp[tid] + rp[4 + tid]) + (rp[8 + tid] + rp[12 + tid]);
    d = rbf(rbf(d) + bf2f(b2[0]));
    const float z = rbf(wide + d);
    out[blk * 4 + tid] = (__bf16)(1.f / (1.f + __expf(-z)));
  }
}

extern "C" void kernel_launch(void* const* d_in, const int* in_sizes, int n_in,
                              void* d_out, int out_size, void* d_ws, size_t ws_size,
                              hipStream_t stream) {
  const int* idx = (const int*)d_in[0];
  const unsigned short* table = (const unsigned short*)d_in[1];
  const unsigned short* xw = (const unsigned short*)d_in[2];
  const unsigned short* ww = (const unsigned short*)d_in[3];
  const unsigned short* bw = (const unsigned short*)d_in[4];
  const unsigned short* W1 = (const unsigned short*)d_in[5];
  const unsigned short* b1 = (const unsigned short*)d_in[6];
  const unsigned short* W2 = (const unsigned short*)d_in[7];
  const unsigned short* b2 = (const unsigned short*)d_in[8];
  float* slab = (float*)d_ws;
  (void)hipFuncSetAttribute((const void*)gemm_kernel, hipFuncAttributeMaxDynamicSharedMemorySize, LDS_BYTES);
  gemm_kernel<<<256, 256, LDS_BYTES, stream>>>(idx, table, W1, slab);
  finish_kernel<<<256, 256, 0, stream>>>(slab, xw, ww, bw, b1, W2, b2, (__bf16*)d_out);
}
```

```cpp
#include <hip/hip_runtime.h>
#include <hip/hip_bf16.h>

typedef short    bf16x8 __attribute__((ext_vector_type(8)));
typedef float    f32x16 __attribute__((ext_vector_type(16)));
typedef float    f32x4  __attribute__((ext_vector_type(4)));
typedef unsigned u32x4  __attribute__((ext_vector_type(4)));
typedef unsigned u32x2  __attribute__((ext_vector_type(2)));
#define LAS __attribute__((address_space(3)))
#define SB() __builtin_amdgcn_sched_barrier(0)

constexpr int NB_ = 1024, NF = 100, ND = 128, NV = 1000, NW = 13, NH = 512;
constexpr int TPITCH   = 80;
constexpr int TBL_LDS  = 80 * 1024;
constexpr int IDX_WAVE = 14 * 1024;
constexpr int LDS_BYTES = TBL_LDS + 4 * IDX_WAVE;
constexpr int SRD3 = 0x00020000;

__device__ __forceinline__ float rbf(float x) { return (float)(__bf16)x; }
__device__ __forceinline__ float bf2f(unsigned short h) { return __uint_as_float((unsigned)h << 16); }

__device__ __forceinline__ void dma16(__amdgpu_buffer_rsrc_t rs, unsigned ldsoff, int voff, int soff) {
  __builtin_amdgcn_raw_ptr_buffer_load_lds(rs, (LAS void*)(size_t)ldsoff, 16, voff, soff, 0, 0);
}
__device__ __forceinline__ u32x4 lds128(unsigned a) { return *(const LAS u32x4*)(size_t)a; }

struct St {
  f32x16 acc[4][2];
  u32x4  A[2][4][2];
  u32x4  B[2][2][2];
  u32x2  W[4][8];
  u32x4  I[2][4];
};

template <int CUR> __device__ __forceinline__ void mf(St& c, int k) {
  const int s = k >> 3, mb = (k >> 1) & 3, nb = k & 1;
  c.acc[mb][nb] = __builtin_amdgcn_mfma_f32_32x32x16_bf16(__builtin_bit_cast(bf16x8, c.A[CUR][mb][s]),
                                                          __builtin_bit_cast(bf16x8, c.B[CUR][s][nb]), c.acc[mb][nb], 0, 0, 0);
}
template <int NX, int SL> __device__ __forceinline__ void swp(St& c, int j) {
  auto r = __builtin_amdgcn_permlane16_swap(c.W[SL][j][0], c.W[SL][j][1], false, false);
  c.W[SL][j][0] = r[0]; c.W[SL][j][1] = r[1];
}
template <int NX, int SL> __device__ __forceinline__ void prm(St& c, int i, int s) {
  c.B[NX][s][0][i] = __builtin_amdgcn_perm(c.W[SL][2 * i + 1][s], c.W[SL][2 * i][s], 0x05040100u);
  c.B[NX][s][1][i] = __builtin_amdgcn_perm(c.W[SL][2 * i + 1][s], c.W[SL][2 * i][s], 0x07060302u);
}
template <int SL> __device__ __forceinline__ void wld(St& c, int j, __amdgpu_buffer_rsrc_t rs, int voff, int soff) {
  c.W[SL][j] = __builtin_amdgcn_raw_buffer_load_b64(rs, voff + (j & 3) * 1024, soff + (j >> 2) * 4096, 0);
}

template <int U, bool MF, bool PF>
__device__ __forceinline__ void step(St& c, unsigned g16, unsigned iaddr, __amdgpu_buffer_rsrc_t rsW, int wv, int soff) {
  constexpr int CUR = U & 1, NX = CUR ^ 1, SL = (U + 1) & 3;
  constexpr int IB = (U == 3) ? 1 : 0;
  constexpr int IC = (U + 1) & 3;
  unsigned ad[4];
#pragma unroll
  for (int mb = 0; mb < 4; ++mb) {
    if (MF) mf<CUR>(c, mb);
    if (PF) {
      ad[mb] = __umul24(c.I[IB][mb][IC], TPITCH) + g16;
      c.A[NX][mb][0] = lds128(ad[mb]);
      c.A[NX][mb][1] = lds128(ad[mb] + 32);
    }
    SB();
  }
#pragma unroll
  for (int i = 0; i < 4; ++i) {
    if (MF) mf<CUR>(c, 4 + 3 * i);
    if (PF) { swp<NX, SL>(c, 2 * i); swp<NX, SL>(c, 2 * i + 1);
              if (U == 0) c.I[1][i] = lds128(iaddr + i * 3584); }
    SB();
    if (MF) mf<CUR>(c, 5 + 3 * i);
    if (PF) { prm<NX, SL>(c, i, 0); prm<NX, SL>(c, i, 1); }
    SB();
    if (MF) mf<CUR>(c, 6 + 3 * i);
    if (PF) { wld<SL>(c, 2 * i, rsW, wv, soff); wld<SL>(c, 2 * i + 1, rsW, wv, soff); }
    SB();
  }
}

#define TS(i) do { SB(); tsr[i] = __builtin_amdgcn_s_memrealtime(); SB(); } while (0)
__global__ void __launch_bounds__(256, 1)
gemm_kernel(const int* __restrict__ idx, const unsigned short* __restrict__ table,
            const unsigned short* __restrict__ W1, float* __restrict__ slab) {
  extern __shared__ __attribute__((aligned(16))) char smem[];
  const unsigned lbase = (unsigned)(size_t)(LAS char*)smem;
  const int tid = threadIdx.x, lane = tid & 63;
  const int w = __builtin_amdgcn_readfirstlane(tid >> 6);
  const int bid = blockIdx.x;
  const int xcd = bid & 7, jj = bid >> 3;
  const int q = xcd >> 1, nt = 4 * (xcd & 1) + (jj & 3), mt = jj >> 2;

  const __amdgpu_buffer_rsrc_t rsW = __builtin_amdgcn_make_buffer_rsrc(
      (void*)(W1 + ((size_t)(25 * w * ND + 32 * q) * NH + nt * 64)), (short)0, (24 * ND + 32) * 1024 - 896, SRD3);
  const __amdgpu_buffer_rsrc_t rsT = __builtin_amdgcn_make_buffer_rsrc((void*)table, (short)0, NV * ND * 2, SRD3);
  const __amdgpu_buffer_rsrc_t rsI = __builtin_amdgcn_make_buffer_rsrc((void*)(idx + mt * 128 * NF), (short)0, 128 * NF * 4, SRD3);

  St c;
  unsigned long long tsr[8];
  TS(0);
  const int c16 = lane & 15, h = (lane >> 4) & 1, g = lane >> 5, r = lane & 31;
  const int wv = (8 * (2 * h + g)) * 1024 + c16 * 8;

#pragma unroll
  for (int j = 0; j < 8; ++j) wld<0>(c, j, rsW, wv, 0);
#pragma unroll
  for (int u = 0; u < 5; ++u) {
    const int s = u * 64 + lane, row = s / 5, ch = s - row * 5;
    const int v = (ch < 4) ? row * 256 + ch * 16 : 0x40000000;
#pragma unroll
    for (int k = 0; k < 4; ++k)
      dma16(rsT, lbase + ((w * 4 + k) * 5 + u) * 1024, v, (w * 4 + k) * 16384 + q * 64);
  }
#pragma unroll
  for (int u = 0; u < 7; ++u) {
    const int s = u * 64 + lane, lr = s / 7, p = s - lr * 7;
    const int v = (lr ^ (32 * (w & 1))) * 400 + p * 16;
#pragma unroll
    for (int hf = 0; hf < 2; ++hf)
      dma16(rsI, lbase + TBL_LDS + w * IDX_WAVE + (hf * 7 + u) * 1024, v, (hf ^ (w >> 1)) * 25600 + 100 * w);
  }
#pragma unroll
  for (int j = 0; j < 8; ++j) wld<1>(c, j, rsW, wv, 1 * 131072);
#pragma unroll
  for (int j = 0; j < 8; ++j) wld<2>(c, j, rsW, wv, 2 * 131072);
#pragma unroll
  for (int j = 0; j < 8; ++j) wld<3>(c, j, rsW, wv, 3 * 131072);
#pragma unroll
  for (int mb = 0; mb < 4; ++mb)
#pragma unroll
    for (int nb = 0; nb < 2; ++nb)
#pragma unroll
      for (int e = 0; e < 16; ++e) c.acc[mb][nb][e] = 0.f;
  TS(1);
  asm volatile("s_waitcnt vmcnt(24)" ::: "memory");
  __builtin_amdgcn_s_barrier();
  TS(2);
  asm volatile("" ::: "memory");

  const unsigned g16 = lbase + g * 16;
  unsigned iaddr = lbase + TBL_LDS + w * IDX_WAVE + r * 112;
#pragma unroll
  for (int mb = 0; mb < 4; ++mb) c.I[1][mb] = lds128(iaddr + mb * 3584);
  iaddr += 16;
  SB();
  step<3, false, true>(c, g16, iaddr, rsW, wv, 4 * 131072);

  int soff = 5 * 131072;
#pragma unroll 1
  for (int p = 0; p < 6; ++p) {
#pragma unroll
    for (int mb = 0; mb < 4; ++mb) c.I[0][mb] = c.I[1][mb];
    step<0, true, true>(c, g16, iaddr, rsW, wv, soff);
    step<1, true, true>(c, g16, iaddr, rsW, wv, soff + 131072);
    step<2, true, true>(c, g16, iaddr, rsW, wv, soff + 2 * 131072);
    step<3, true, true>(c, g16, iaddr, rsW, wv, soff + 3 * 131072);
    soff += 4 * 131072;
    iaddr += 16;
  }
  step<0, true, false>(c, g16, iaddr, rsW, wv, soff);

  TS(4);
  __syncthreads();
#pragma unroll
  for (int i = 1; i < 4; ++i) {
    const unsigned d = lbase + (((w ^ i) * 3 + (i - 1)) * 8192) + lane * 16;
#pragma unroll
    for (int nb = 0; nb < 2; ++nb)
#pragma unroll
      for (int qd = 0; qd < 4; ++qd) {
        f32x4 v = {c.acc[i][nb][4 * qd], c.acc[i][nb][4 * qd + 1], c.acc[i][nb][4 * qd + 2], c.acc[i][nb][4 * qd + 3]};
        *(LAS f32x4*)(size_t)(d + (nb * 4 + qd) * 1024) = v;
      }
  }
  __syncthreads();
  const __amdgpu_buffer_rsrc_t rsS = __builtin_amdgcn_make_buffer_rsrc(
      (void*)((char*)slab + (size_t)(mt * 4 + w) * 8 * 32768 + q * 8192 + nt * 1024), (short)0, 8 * 32768, SRD3);
  const unsigned sa = lbase + w * 3 * 8192 + lane * 16;
  const int sv = g * 32768 + r * 16;
#pragma unroll
  for (int nb = 0; nb < 2; ++nb)
#pragma unroll
    for (int qd = 0; qd < 4; ++qd) {
      f32x4 v = {c.acc[0][nb][4 * qd], c.acc[0][nb][4 * qd + 1], c.acc[0][nb][4 * qd + 2], c.acc[0][nb][4 * qd + 3]};
#pragma unroll
      for (int i = 0; i < 3; ++i) v += *(const LAS f32x4*)(size_t)(sa + i * 8192 + (nb * 4 + qd) * 1024);
      __builtin_amdgcn_raw_buffer_store_b128(__builtin_bit_cast(u32x4, v), rsS, sv + qd * 65536 + nb * 512, 0, 0);
    }
  asm volatile("s_waitcnt vmcnt(0)" ::: "memory");
  TS(7);
  { unsigned long long lim = tsr[7] + 8 * (tsr[0] - tsr[0]); while (__builtin_amdgcn_s_memrealtime() < lim) __builtin_amdgcn_s_sleep(2); }
}

__global__ void __launch_bounds__(256)
finish_kernel(const float* __restrict__ slab, const unsigned short* __restrict__ xw, const unsigned short* __restrict__ ww,
              const unsigned short* __restrict__ bw, const unsigned short* __restrict__ b1, const unsigned short* __restrict__ W2,
              const unsigned short* __restrict__ b2, __bf16* __restrict__ out) {
  __shared__ f32x4 red[4];
  const int blk = blockIdx.x, tid = threadIdx.x, lane = tid & 63, w = tid >> 6;
  const f32x4* sp = (const f32x4*)slab + (size_t)blk * 2048;
  f32x4 v[2][4];
#pragma unroll
  for (int i = 0; i < 2; ++i)
#pragma unroll
    for (int q = 0; q < 4; ++q) v[i][q] = sp[q * 512 + i * 256 + tid];
  float wide = 0.f;
  if (tid < 4) {
    const unsigned short* x = xw + (blk * 4 + tid) * NW;
#pragma unroll
    for (int k = 0; k < NW; ++k) wide += bf2f(x[k]) * bf2f(ww[k]);
    wide = rbf(rbf(wide) + bf2f(bw[0]));
  }
  f32x4 a = {0.f, 0.f, 0.f, 0.f};
#pragma unroll
  for (int i = 0; i < 2; ++i) {
    const int e = i * 256 + tid, cc = e & 31;
    const int n = (e >> 6) * 64 + 4 * (cc & 15) + 2 * (cc >> 4) + ((e >> 5) & 1);
    const float bb = bf2f(b1[n]), w2 = bf2f(W2[n]);
    const f32x4 s = (v[i][0] + v[i][1]) + (v[i][2] + v[i][3]);
#pragma unroll
    for (int k = 0; k < 4; ++k) {
      float hh = rbf(rbf(s[k]) + bb);
      hh = hh > 0.f ? hh : hh * 0.f;
      a[k] += hh * w2;
    }
  }
#pragma unroll
  for (int o = 32; o >= 1; o >>= 1)
#pragma unroll
    for (int k = 0; k < 4; ++k) a[k] += __shfl_xor(a[k], o, 64);
  if (lane == 0) red[w] = a;
  __syncthreads();
  if (tid < 4) {
    const float* rp = (const float*)red;
    float d = (rp[tid] + rp[4 + tid]) + (rp[8 + tid] + rp[12 + tid]);
    d = rbf(rbf(d) + bf2f(b2[0]));
    const float z = rbf(wide + d);
    out[blk * 4 + tid] = (__bf16)(1.f / (1.f + __expf(-z)));
  }
}

extern "C" void kernel_launch(void* const* d_in, const int* in_sizes, int n_in,
                              void* d_out, int out_size, void* d_ws, size_t ws_size,
                              hipStream_t stream) {
  const int* idx = (const int*)d_in[0];
  const unsigned short* table = (const unsigned short*)d_in[1];
  const unsigned short* xw = (const unsigned short*)d_in[2];
  const unsigned short* ww = (const unsigned short*)d_in[3];
  const unsigned short* bw = (const unsigned short*)d_in[4];
  const unsigned short* W1 = (const unsigned short*)d_in[5];
  const unsigned short* b1 = (const unsigned short*)d_in[6];
  const unsigned short* W2 = (const unsigned short*)d_in[7];
  const unsigned short* b2 = (const unsigned short*)d_in[8];
  float* slab = (float*)d_ws;
  (void)hipFuncSetAttribute((const void*)gemm_kernel, hipFuncAttributeMaxDynamicSharedMemorySize, LDS_BYTES);
  gemm_kernel<<<256, 256, LDS_BYTES, stream>>>(idx, table, W1, slab);
  finish_kernel<<<256, 256, 0, stream>>>(slab, xw, ww, bw, b1, W2, b2, (__bf16*)d_out);
}
```

```cpp
#include <hip/hip_runtime.h>
#include <hip/hip_bf16.h>

typedef short    bf16x8 __attribute__((ext_vector_type(8)));
typedef float    f32x16 __attribute__((ext_vector_type(16)));
typedef float    f32x4  __attribute__((ext_vector_type(4)));
typedef unsigned u32x4  __attribute__((ext_vector_type(4)));
typedef unsigned u32x2  __attribute__((ext_vector_type(2)));
#define LAS __attribute__((address_space(3)))
#define SB() __builtin_amdgcn_sched_barrier(0)

constexpr int NB_ = 1024, NF = 100, ND = 128, NV = 1000, NW = 13, NH = 512;
constexpr int TPITCH   = 80;
constexpr int TBL_LDS  = 80 * 1024;
constexpr int IDX_WAVE = 14 * 1024;
constexpr int MISC_LDS = TBL_LDS + 4 * IDX_WAVE;
constexpr int LDS_BYTES = MISC_LDS + 128;
constexpr int SLAB_BYTES = 8 << 20;
constexpr int SC1 = 16;
constexpr int SC1V = 16;
constexpr int SRD3 = 0x00020000;

__device__ __forceinline__ float rbf(float x) { return (float)(__bf16)x; }
__device__ __forceinline__ float bf2f(unsigned short h) { return __uint_as_float((unsigned)h << 16); }

__device__ __forceinline__ void dma16(__amdgpu_buffer_rsrc_t rs, unsigned ldsoff, int voff, int soff) {
  __builtin_amdgcn_raw_ptr_buffer_load_lds(rs, (LAS void*)(size_t)ldsoff, 16, voff, soff, 0, 0);
}
template <int C> __device__ __forceinline__ float dpp_add(float x) {
  return x + __uint_as_float(__builtin_amdgcn_update_dpp(0, __float_as_uint(x), C, 0xf, 0xf, true));
}
__device__ __forceinline__ float row_sum16(float x) {
  x = dpp_add<0xB1>(x);
  x = dpp_add<0x4E>(x);
  x = dpp_add<0x141>(x);
  x = dpp_add<0x140>(x);
  return x;
}
__device__ __forceinline__ u32x4 lds128(unsigned a) { return *(const LAS u32x4*)(size_t)a; }

struct St {
  f32x16 acc[4][2];
  u32x4  A[2][4][2];
  u32x4  B[2][2][2];
  u32x2  W[4][8];
  u32x4  I[2][4];
};

template <int CUR> __device__ __forceinline__ void mf(St& c, int k) {
  const int s = k >> 3, mb = (k >> 1) & 3, nb = k & 1;
  c.acc[mb][nb] = __builtin_amdgcn_mfma_f32_32x32x16_bf16(__builtin_bit_cast(bf16x8, c.A[CUR][mb][s]),
                                                          __builtin_bit_cast(bf16x8, c.B[CUR][s][nb]), c.acc[mb][nb], 0, 0, 0);
}
template <int NX, int SL> __device__ __forceinline__ void swp(St& c, int j) {
  auto r = __builtin_amdgcn_permlane16_swap(c.W[SL][j][0], c.W[SL][j][1], false, false);
  c.W[SL][j][0] = r[0]; c.W[SL][j][1] = r[1];
}
template <int NX, int SL> __device__ __forceinline__ void prm(St& c, int i, int s) {
  c.B[NX][s][0][i] = __builtin_amdgcn_perm(c.W[SL][2 * i + 1][s], c.W[SL][2 * i][s], 0x05040100u);
  c.B[NX][s][1][i] = __builtin_amdgcn_perm(c.W[SL][2 * i + 1][s], c.W[SL][2 * i][s], 0x07060302u);
}
template <int SL> __device__ __forceinline__ void wld(St& c, int j, __amdgpu_buffer_rsrc_t rs, int voff, int soff) {
  c.W[SL][j] = __builtin_amdgcn_raw_buffer_load_b64(rs, voff + (j & 3) * 1024, soff + (j >> 2) * 4096, 0);
}

template <int U, bool MF, bool PF>
__device__ __forceinline__ void step(St& c, unsigned g16, unsigned iaddr, __amdgpu_buffer_rsrc_t rsW, int wv, int soff) {
  constexpr int CUR = U & 1, NX = CUR ^ 1, SL = (U + 1) & 3;
  constexpr int IB = (U == 3) ? 1 : 0;
  constexpr int IC = (U + 1) & 3;
  unsigned ad[4];
#pragma unroll
  for (int mb = 0; mb < 4; ++mb) {
    if (MF) mf<CUR>(c, mb);
    if (PF) {
      ad[mb] = __umul24(c.I[IB][mb][IC], TPITCH) + g16;
      c.A[NX][mb][0] = lds128(ad[mb]);
      c.A[NX][mb][1] = lds128(ad[mb] + 32);
    }
    SB();
  }
#pragma unroll
  for (int i = 0; i < 4; ++i) {
    if (MF) mf<CUR>(c, 4 + 3 * i);
    if (PF) { swp<NX, SL>(c, 2 * i); swp<NX, SL>(c, 2 * i + 1);
              if (U == 0) c.I[1][i] = lds128(iaddr + i * 3584); }
    SB();
    if (MF) mf<CUR>(c, 5 + 3 * i);
    if (PF) { prm<NX, SL>(c, i, 0); prm<NX, SL>(c, i, 1); }
    SB();
    if (MF) mf<CUR>(c, 6 + 3 * i);
    if (PF) { wld<SL>(c, 2 * i, rsW, wv, soff); wld<SL>(c, 2 * i + 1, rsW, wv, soff); }
    SB();
  }
}

__global__ void __launch_bounds__(256, 1)
fused_kernel(const int* __restrict__ idx, const unsigned short* __restrict__ table,
             const unsigned short* __restrict__ W1, char* slab,
             const unsigned short* __restrict__ xw, const unsigned short* __restrict__ ww,
             const unsigned short* __restrict__ bw, const unsigned short* __restrict__ b1,
             const unsigned short* __restrict__ W2, const unsigned short* __restrict__ b2, __bf16* __restrict__ out) {
  extern __shared__ __attribute__((aligned(16))) char smem[];
  const unsigned lbase = (unsigned)(size_t)(LAS char*)smem;
  const int tid = threadIdx.x, lane = tid & 63;
  const int w = __builtin_amdgcn_readfirstlane(tid >> 6);
  const int bid = blockIdx.x;
  const int xcd = bid & 7, jj = bid >> 3;
  const int q = xcd >> 1, nt = 4 * (xcd & 1) + (jj & 3), mt = jj >> 2;

  const __amdgpu_buffer_rsrc_t rsW = __builtin_amdgcn_make_buffer_rsrc(
      (void*)(W1 + ((size_t)(25 * w * ND + 32 * q) * NH + nt * 64)), (short)0, (24 * ND + 32) * 1024 - 896, SRD3);
  const __amdgpu_buffer_rsrc_t rsT = __builtin_amdgcn_make_buffer_rsrc((void*)table, (short)0, NV * ND * 2, SRD3);
  const __amdgpu_buffer_rsrc_t rsI = __builtin_amdgcn_make_buffer_rsrc((void*)(idx + mt * 128 * NF), (short)0, 128 * NF * 4, SRD3);

  const int me = q * 8 + nt;
  const __amdgpu_buffer_rsrc_t rsF = __builtin_amdgcn_make_buffer_rsrc((void*)(slab + SLAB_BYTES + mt * 4096), (short)0, 4096, SRD3);
  unsigned f0 = 0;
  if (w == 0) f0 = __builtin_amdgcn_raw_buffer_load_b32(rsF, me * 128 + (lane & 31) * 4, 0, SC1);

  St c;
  const int c16 = lane & 15, h = (lane >> 4) & 1, g = lane >> 5, r = lane & 31;
  const int wv = (8 * (2 * h + g)) * 1024 + c16 * 8;

#pragma unroll
  for (int j = 0; j < 8; ++j) wld<0>(c, j, rsW, wv, 0);
#pragma unroll
  for (int u = 0; u < 5; ++u) {
    const int s = u * 64 + lane, row = s / 5, ch = s - row * 5;
    const int v = (ch < 4) ? row * 256 + ch * 16 : 0x40000000;
#pragma unroll
    for (int k = 0; k < 4; ++k)
      dma16(rsT, lbase + ((w * 4 + k) * 5 + u) * 1024, v, (w * 4 + k) * 16384 + q * 64);
  }
#pragma unroll
  for (int u = 0; u < 7; ++u) {
    const int s = u * 64 + lane, lr = s / 7, p = s - lr * 7;
    const int v = (lr ^ (32 * (w & 1))) * 400 + p * 16;
#pragma unroll
    for (int hf = 0; hf < 2; ++hf)
      dma16(rsI, lbase + TBL_LDS + w * IDX_WAVE + (hf * 7 + u) * 1024, v, (hf ^ (w >> 1)) * 25600 + 100 * w);
  }
#pragma unroll
  for (int j = 0; j < 8; ++j) wld<1>(c, j, rsW, wv, 1 * 131072);
#pragma unroll
  for (int j = 0; j < 8; ++j) wld<2>(c, j, rsW, wv, 2 * 131072);
#pragma unroll
  for (int j = 0; j < 8; ++j) wld<3>(c, j, rsW, wv, 3 * 131072);
#pragma unroll
  for (int mb = 0; mb < 4; ++mb)
#pragma unroll
    for (int nb = 0; nb < 2; ++nb)
#pragma unroll
      for (int e = 0; e < 16; ++e) c.acc[mb][nb][e] = 0.f;
  asm volatile("s_waitcnt vmcnt(24)" ::: "memory");
  __builtin_amdgcn_s_barrier();
  asm volatile("" ::: "memory");
  if (w == 0) {
    const unsigned t = (unsigned)__builtin_amdgcn_ballot_w64(((f0 >> (lane & 31)) & 1) == 0);
    if (lane == 0) {
      __builtin_amdgcn_raw_buffer_store_b32(t, rsF, me * 132, 0, SC1);
      *(LAS unsigned*)(size_t)(lbase + MISC_LDS) = t;
    }
  }

  const unsigned g16 = lbase + g * 16;
  unsigned iaddr = lbase + TBL_LDS + w * IDX_WAVE + r * 112;
#pragma unroll
  for (int mb = 0; mb < 4; ++mb) c.I[1][mb] = lds128(iaddr + mb * 3584);
  iaddr += 16;
  SB();
  step<3, false, true>(c, g16, iaddr, rsW, wv, 4 * 131072);

  int soff = 5 * 131072;
#pragma unroll 1
  for (int p = 0; p < 6; ++p) {
#pragma unroll
    for (int mb = 0; mb < 4; ++mb) c.I[0][mb] = c.I[1][mb];
    step<0, true, true>(c, g16, iaddr, rsW, wv, soff);
    step<1, true, true>(c, g16, iaddr, rsW, wv, soff + 131072);
    step<2, true, true>(c, g16, iaddr, rsW, wv, soff + 2 * 131072);
    step<3, true, true>(c, g16, iaddr, rsW, wv, soff + 3 * 131072);
    soff += 4 * 131072;
    iaddr += 16;
  }
  step<0, true, false>(c, g16, iaddr, rsW, wv, soff);

  asm volatile("s_waitcnt vmcnt(0) lgkmcnt(0)" ::: "memory");
  __builtin_amdgcn_s_barrier();
  asm volatile("" ::: "memory");
  const int nv = (lane < 8) ? (8 * w + lane) * 132 : 0x40000000;
  const int ev = (lane < 8 && 8 * w + lane != me) ? (8 * w + lane) * 128 + me * 4 : 0x40000000;
  const unsigned nonce = __builtin_amdgcn_raw_buffer_load_b32(rsF, nv, 0, SC1V);
  const int blk = mt * 32 + me;
  unsigned short bbv[2], w2v[2];
#pragma unroll
  for (int i = 0; i < 2; ++i) {
    const int n = (2 * w + i) * 64 + 4 * c16 + 2 * h + g;
    bbv[i] = b1[n]; w2v[i] = W2[n];
  }
  const int kx = c16 < NW ? c16 : 0;
  const unsigned short xv = xw[(blk * 4 + (lane >> 4)) * NW + kx], wv_ = ww[kx], bwv = bw[0], b2v = b2[0];
#pragma unroll
  for (int i = 1; i < 4; ++i) {
    const unsigned d = lbase + (((w ^ i) * 3 + (i - 1)) * 8192) + lane * 16;
#pragma unroll
    for (int nb = 0; nb < 2; ++nb)
#pragma unroll
      for (int qd = 0; qd < 4; ++qd) {
        f32x4 v = {c.acc[i][nb][4 * qd], c.acc[i][nb][4 * qd + 1], c.acc[i][nb][4 * qd + 2], c.acc[i][nb][4 * qd + 3]};
        *(LAS f32x4*)(size_t)(d + (nb * 4 + qd) * 1024) = v;
      }
  }
  asm volatile("s_waitcnt lgkmcnt(0)" ::: "memory");
  __builtin_amdgcn_s_barrier();
  asm volatile("" ::: "memory");
  const unsigned T = *(const LAS unsigned*)(size_t)(lbase + MISC_LDS);
  const __amdgpu_buffer_rsrc_t rsS = __builtin_amdgcn_make_buffer_rsrc(
      (void*)(slab + (size_t)(mt * 4 + w) * 8 * 32768 + q * 8192 + nt * 1024), (short)0, 8 * 32768, SRD3);
  const unsigned sa = lbase + w * 3 * 8192 + lane * 16;
  const int sv = g * 32768 + r * 16;
  f32x4 rd[8][3];
#pragma unroll
  for (int j = 0; j < 8; ++j)
#pragma unroll
    for (int i = 0; i < 3; ++i) rd[j][i] = *(const LAS f32x4*)(size_t)(sa + i * 8192 + j * 1024);
  SB();
#pragma unroll
  for (int j = 0; j < 8; ++j) {
    const int nb = j >> 2, qd = j & 3;
    f32x4 v = {c.acc[0][nb][4 * qd], c.acc[0][nb][4 * qd + 1], c.acc[0][nb][4 * qd + 2], c.acc[0][nb][4 * qd + 3]};
    v = (v + rd[j][0]) + (rd[j][1] + rd[j][2]);
    __builtin_amdgcn_raw_buffer_store_b128(__builtin_bit_cast(u32x4, v), rsS, sv + qd * 65536 + nb * 512, 0, SC1);
    SB();
  }
  const float wide = row_sum16(c16 < NW ? bf2f(xv) * bf2f(wv_) : 0.f);
  const float bb[2] = {bf2f(bbv[0]), bf2f(bbv[1])}, w2[2] = {bf2f(w2v[0]), bf2f(w2v[1])};
  const float bwf = bf2f(bwv), b2f_ = bf2f(b2v);
  asm volatile("s_waitcnt vmcnt(0)" ::: "memory");
  __builtin_amdgcn_raw_buffer_store_b32(nonce, rsF, ev, 0, SC1);
  __builtin_amdgcn_s_barrier();
  asm volatile("" ::: "memory");
#pragma unroll 1
  for (int it = 0; it < (1 << 16); ++it) {
    const unsigned f = __builtin_amdgcn_raw_buffer_load_b32(rsF, me * 128 + (lane & 31) * 4, 0, SC1V);
    if (__builtin_amdgcn_ballot_w64(f != T) == 0) break;
    asm volatile("" ::: "memory");
  }
  asm volatile("" ::: "memory");
  const __amdgpu_buffer_rsrc_t rsR = __builtin_amdgcn_make_buffer_rsrc((void*)(slab + (size_t)blk * 32768), (short)0, 32768, SRD3);
  f32x4 v[2][4];
#pragma unroll
  for (int i = 0; i < 2; ++i)
#pragma unroll
    for (int k = 0; k < 4; ++k)
      v[i][k] = __builtin_bit_cast(f32x4, __builtin_amdgcn_raw_buffer_load_b128(rsR, w * 2048 + lane * 16 + i * 1024, k * 8192, SC1));
  const unsigned nonce2 = __builtin_amdgcn_raw_buffer_load_b32(rsF, nv, 0, SC1V);
  float a[4] = {0.f, 0.f, 0.f, 0.f};
#pragma unroll
  for (int i = 0; i < 2; ++i) {
    const f32x4 s = (v[i][0] + v[i][1]) + (v[i][2] + v[i][3]);
#pragma unroll
    for (int k = 0; k < 4; ++k) {
      float hh = rbf(rbf(s[k]) + bb[i]);
      hh = hh > 0.f ? hh : hh * 0.f;
      a[k] += hh * w2[i];
    }
  }
  if (nonce2 != nonce) __builtin_amdgcn_raw_buffer_store_b32(nonce2, rsF, ev, 0, SC1);
  auto p02 = __builtin_amdgcn_permlane32_swap(__float_as_uint(a[0]), __float_as_uint(a[2]), false, false);
  auto p13 = __builtin_amdgcn_permlane32_swap(__float_as_uint(a[1]), __float_as_uint(a[3]), false, false);
  const float s02 = __uint_as_float(p02[0]) + __uint_as_float(p02[1]);
  const float s13 = __uint_as_float(p13[0]) + __uint_as_float(p13[1]);
  auto pq = __builtin_amdgcn_permlane16_swap(__float_as_uint(s02), __float_as_uint(s13), false, false);
  const float dsum = row_sum16(__uint_as_float(pq[0]) + __uint_as_float(pq[1]));
  if (c16 == 0) *(LAS float*)(size_t)(lbase + MISC_LDS + 64 + (lane >> 4) * 16 + w * 4) = dsum;
  asm volatile("s_waitcnt lgkmcnt(0)" ::: "memory");
  __builtin_amdgcn_s_barrier();
  asm volatile("" ::: "memory");
  if (w == 0 && c16 == 0) {
    const f32x4 rp = *(const LAS f32x4*)(size_t)(lbase + MISC_LDS + 64 + (lane >> 4) * 16);
    float d = (rp[0] + rp[1]) + (rp[2] + rp[3]);
    d = rbf(rbf(d) + b2f_);
    const float z = rbf(rbf(rbf(wide) + bwf) + d);
    out[blk * 4 + (lane >> 4)] = (__bf16)(1.f / (1.f + __expf(-z)));
  }
}

extern "C" void kernel_launch(void* const* d_in, const int* in_sizes, int n_in,
                              void* d_out, int out_size, void* d_ws, size_t ws_size,
                              hipStream_t stream) {
  const int* idx = (const int*)d_in[0];
  const unsigned short* table = (const unsigned short*)d_in[1];
  const unsigned short* xw = (const unsigned short*)d_in[2];
  const unsigned short* ww = (const unsigned short*)d_in[3];
  const unsigned short* bw = (const unsigned short*)d_in[4];
  const unsigned short* W1 = (const unsigned short*)d_in[5];
  const unsigned short* b1 = (const unsigned short*)d_in[6];
  const unsigned short* W2 = (const unsigned short*)d_in[7];
  const unsigned short* b2 = (const unsigned short*)d_in[8];
  (void)hipFuncSetAttribute((const void*)fused_kernel, hipFuncAttributeMaxDynamicSharedMemorySize, LDS_BYTES);
  fused_kernel<<<256, 256, LDS_BYTES, stream>>>(idx, table, W1, (char*)d_ws, xw, ww, bw, b1, W2, b2, (__bf16*)d_out);
}
```

```cpp
#include <hip/hip_runtime.h>
#include <hip/hip_bf16.h>

typedef short    bf16x8 __attribute__((ext_vector_type(8)));
typedef __bf16   bf16x2 __attribute__((ext_vector_type(2)));
typedef float    f32x4  __attribute__((ext_vector_type(4)));
typedef float    f32x2  __attribute__((ext_vector_type(2)));
typedef unsigned u32x4  __attribute__((ext_vector_type(4)));
typedef unsigned u32x2  __attribute__((ext_vector_type(2)));
#define LAS __attribute__((address_space(3)))
#define SB() __builtin_amdgcn_sched_barrier(0)
#define FENCE() asm volatile("" ::: "memory")

constexpr int NF = 100, ND = 128, NV = 1000, NW = 13, NH = 512;
constexpr int TPIECE = 1040;
constexpr int IDX_LDS = 64 * TPIECE;
constexpr int X1_LDS = IDX_LDS;
constexpr int X0_OFF = 52 * 1024;
constexpr int LDS_BYTES = X1_LDS + X0_OFF + 24576;
constexpr int FROW = ND * NH * 2;
constexpr int SC1 = 16;
constexpr int SRD3 = 0x00020000;
constexpr int OOB = 0x40000000;

__device__ __forceinline__ float rbf(float x) { return (float)(__bf16)x; }
__device__ __forceinline__ float bf2f(unsigned short h) { return __uint_as_float((unsigned)h << 16); }
template <int C> __device__ __forceinline__ float dpp_add(float x) {
  return x + __uint_as_float(__builtin_amdgcn_update_dpp(0, __float_as_uint(x), C, 0xf, 0xf, true));
}
__device__ __forceinline__ float row_sum16(float x) {
  x = dpp_add<0xB1>(x);
  x = dpp_add<0x4E>(x);
  x = dpp_add<0x141>(x);
  x = dpp_add<0x140>(x);
  return x;
}
__device__ __forceinline__ u32x4 lds128(unsigned a) { return *(const LAS u32x4*)(size_t)a; }
__device__ __forceinline__ f32x4 ldsf4(unsigned a) { return *(const LAS f32x4*)(size_t)a; }
__device__ __forceinline__ unsigned lds32(unsigned a) { return *(const LAS unsigned*)(size_t)a; }
__device__ __forceinline__ unsigned taddr(unsigned v) { return (v << 6) + (v & ~15u); }
__device__ __forceinline__ f32x4 mfma(u32x4 a, u32x4 b, f32x4 c) {
  return __builtin_amdgcn_mfma_f32_16x16x32_bf16(__builtin_bit_cast(bf16x8, a), __builtin_bit_cast(bf16x8, b), c, 0, 0, 0);
}

__device__ __forceinline__ void mfa(f32x4& c, const u32x4& a, const u32x4& b) {
  asm volatile("v_mfma_f32_16x16x32_bf16 %0, %1, %2, %0" : "+a"(c) : "v"(a), "v"(b));
  SB();
}

struct St {
  f32x4 acc[8][4];
  u32x4 T[8];
  u32x4 F[2][4];
  u32x2 R[4][8];
  unsigned ad[8], I[8];
  unsigned selA, selB;
};
__device__ __forceinline__ void prm(const St& c, u32x4* F, const u32x2* R, int i, int h) {
  F[2 * h][i]     = __builtin_amdgcn_perm(R[2 * i + 1][h], R[2 * i][h], c.selA);
  F[2 * h + 1][i] = __builtin_amdgcn_perm(R[2 * i + 1][h], R[2 * i][h], c.selB);
}
__device__ __forceinline__ u32x2 wld(int j, __amdgpu_buffer_rsrc_t rs, int voff, int soff) {
  return __builtin_amdgcn_raw_buffer_load_b64(rs, voff + (j & 3) * 1024, soff + (j >> 2) * 4096, 0);
}

struct Lp {
  int n, f, wb, wv, hv, ev, cn, cm, so;
  unsigned ib0, ib1, ia0, ia1, hs, nn, ee;
};
template <int U>
__device__ __forceinline__ void step(St& c, Lp& l, __amdgpu_buffer_rsrc_t rsW, __amdgpu_buffer_rsrc_t rsF) {
  constexpr int CUR = U & 1, NX = CUR ^ 1, SL = (U + 1) & 3, SN = (U + 2) & 3;
#pragma unroll
  for (int i = 0; i < 4; ++i) {
    const int m0 = 2 * i, m1 = 2 * i + 1;
    mfa(c.acc[m0][0], c.F[CUR][0], c.T[m0]);
    prm(c, c.F[NX], c.R[SL], i, 0);
    SB();
    mfa(c.acc[m0][1], c.F[CUR][1], c.T[m0]);
    prm(c, c.F[NX], c.R[SL], i, 1);
    SB();
    mfa(c.acc[m0][2], c.F[CUR][2], c.T[m0]);
    if (U == 0) c.ad[m0] = taddr(c.I[m0]);
    if (U == 2 && i == 0) l.so = l.n == 2 ? 0 : OOB;
    if (U == 2 && i == 1) l.so = ((18 >> l.n) & 1) ? l.cn : OOB;
    if (U == 2 && i == 2) l.so = l.n == 3 ? l.cm : l.so;
    if (U == 3 && i == 0) l.f = l.f + 1;
    if (U == 3 && i == 1) l.f = l.f == 6 ? 0 : l.f;
    if (U == 3 && i == 2) { l.ia0 = l.ib0 + l.f * 64; l.ia1 = l.ib1 + l.f * 64; asm volatile("" : "+v"(l.ia0), "+v"(l.ia1)); }
    if (U == 2 || U == 3) asm volatile("" : "+s"(l.so), "+s"(l.f));
    SB();
    mfa(c.acc[m0][3], c.F[CUR][3], c.T[m0]);
    c.T[m0] = lds128(c.ad[m0] + 16 * U);
    c.R[SL][m0] = wld(m0, rsW, l.wv, U * 8192);
    SB();
    mfa(c.acc[m1][0], c.F[CUR][0], c.T[m1]);
    if (U == 1) {
      const int m = (i & 1) + 4 * (i >> 1);
      c.I[m] = lds32(((i & 1) ? l.ia1 : l.ia0) + m * 6400);
      c.I[m + 2] = lds32(((i & 1) ? l.ia1 : l.ia0) + (m + 2) * 6400);
    }
    if (U == 2 && i == 0) __builtin_amdgcn_raw_buffer_store_b32(l.hs, rsF, l.ev, l.so, SC1);
    if (U == 2 && i == 1) { l.nn |= l.ee; l.ee = l.hs; asm volatile("" : "+v"(l.nn), "+v"(l.ee)); }
    if (U == 2 && i == 2) l.hs = __builtin_amdgcn_raw_buffer_load_b32(rsF, l.hv, l.so, SC1);
    SB();
    mfa(c.acc[m1][1], c.F[CUR][1], c.T[m1]);
    if (U == 0) c.ad[m1] = taddr(c.I[m1]);
    if (U == 3 && i == 3) { l.n = l.n + 1; asm volatile("" : "+s"(l.n)); }
    SB();
    mfa(c.acc[m1][2], c.F[CUR][2], c.T[m1]);
    c.R[SL][m1] = wld(m1, rsW, l.wv, U * 8192);
    if (U == 3 && i == 3) { SB(); l.wv = l.wb + l.f * (16 * FROW); asm volatile("" : "+v"(l.wv)); }
    SB();
    mfa(c.acc[m1][3], c.F[CUR][3], c.T[m1]);
    c.T[m1] = lds128(c.ad[m1] + 16 * U);
    SB();
    if (i < 3) asm volatile("" :: "v"(c.T[m0 + 2]), "v"(c.T[m1 + 2]), "v"(c.R[SL][m0 + 2]), "v"(c.R[SL][m1 + 2]));
    else       asm volatile("" :: "v"(c.T[0]), "v"(c.T[1]), "v"(c.R[SN][0]), "v"(c.R[SN][1]));
    SB();
  }
}

struct Tl {
  u32x4 G[4][4];
  u32x4 TT[2][2][4];
  f32x4 rd[3][2];
  f32x4 s[2];
  unsigned xa[3], xr;
  int pv;
};
template <int Q> __device__ __forceinline__ void xwrite(St& c, Tl& t, int k, int h) {
  *(LAS f32x4*)(size_t)(t.xa[k - 1] + ((Q & 1) ? 0 : X0_OFF) + h * 1024) = c.acc[2 * Q + (k & 1)][2 * h + (k >> 1)];
}
template <int Q> __device__ __forceinline__ void xread(Tl& t, int k, int h) {
  t.rd[k][h] = ldsf4(t.xr + ((Q & 1) ? 0 : X0_OFF) + k * 2048 + h * 1024);
}
template <int Q> __device__ __forceinline__ void xsum(St& c, Tl& t, int h, int ph) {
  if (ph == 0) { asm volatile("" : "+a"(c.acc[2 * Q][2 * h])); t.s[h] = c.acc[2 * Q][2 * h] + t.rd[0][h]; }
  if (ph == 1) t.rd[1][h] += t.rd[2][h];
  if (ph == 2) t.s[h] += t.rd[1][h];
}
template <int Q> __device__ __forceinline__ void xpub(Tl& t, __amdgpu_buffer_rsrc_t rsS) {
  u32x4 o;
#pragma unroll
  for (int e = 0; e < 4; ++e) {
    const f32x2 p = {t.s[0][e], t.s[1][e]};
    o[e] = min(__builtin_bit_cast(unsigned, __builtin_convertvector(p, bf16x2)), 0xFFFFFFFEu);
  }
  __builtin_amdgcn_raw_buffer_store_b128(o, rsS, t.pv + Q * (1 << 20), 0, SC1);
}
__device__ __forceinline__ void wgbar() {
  asm volatile("s_waitcnt lgkmcnt(0)" ::: "memory");
  __builtin_amdgcn_s_barrier();
  FENCE();
}

template <int Q>
__device__ __forceinline__ void quarter(St& c, Tl& t, __amdgpu_buffer_rsrc_t rsS) {
  constexpr int B = Q & 1;
#pragma unroll
  for (int k = 0; k < 32; ++k) {
    int ch, u, nb;
    if (Q < 3) { ch = k >> 3; u = (k >> 2) & 1; nb = k & 3; }
    else { const int pr = k >> 3; ch = (k >> 1) & 3; u = pr < 2 ? 1 : 0; nb = 2 * (k & 1) + ((pr == 1 || pr == 2) ? 1 : 0); }
    c.acc[2 * Q + u][nb] = mfma(t.G[ch][nb], t.TT[B][u][ch], c.acc[2 * Q + u][nb]);
    SB();
    if constexpr (Q == 0) {
      if (k < 8) prm(c, t.G[2], c.R[3], k >> 1, k & 1);
      if (k >= 14 && k < 22) prm(c, t.G[3], c.R[0], (k - 14) >> 1, k & 1);
      if (k == 8 || k == 9) c.ad[k - 6] = taddr(c.I[k - 6]);
      if (k >= 10 && k < 14) t.TT[1][k & 1][(k - 10) >> 1] = lds128(c.ad[2 + (k & 1)] + 16 * ((k - 10) >> 1));
      if (k >= 22 && k < 26) t.TT[1][k & 1][(k - 18) >> 1] = lds128(c.ad[2 + (k & 1)] + 16 * ((k - 18) >> 1));
      if (k >= 26 && k < 30) c.ad[k - 22] = taddr(c.I[k - 22]);
    } else {
      if (k >= 1 && k < 7) xwrite<Q - 1>(c, t, 1 + (k - 1) / 2, (k - 1) & 1);
      if (k == 9) wgbar();
      if (k >= 10 && k < 13) { xread<Q - 1>(t, k - 10, 0); xread<Q - 1>(t, k - 10, 1); }
      if (Q < 3) {
        if (k >= 13 && k < 21) t.TT[B ^ 1][(k - 13) & 1][(k - 13) >> 1] = lds128(c.ad[2 * Q + 2 + ((k - 13) & 1)] + 16 * ((k - 13) >> 1));
        if (k >= 21 && k < 27) xsum<Q - 1>(c, t, (k - 21) & 1, (k - 21) >> 1);
        if (k == 27) xpub<Q - 1>(t, rsS);
      } else {
        if (k == 13 || k == 14) xwrite<3>(c, t, 1, k - 13);
        if (k == 17 || k == 18) xwrite<3>(c, t, 3, k - 17);
        if (k >= 19 && k < 25) xsum<Q - 1>(c, t, (k - 19) & 1, (k - 19) >> 1);
        if (k == 25 || k == 26) xwrite<3>(c, t, 2, k - 25);
        if (k == 27) xpub<Q - 1>(t, rsS);
        if (k == 30) wgbar();
        if (k == 30) { xread<3>(t, 1, 0); xread<3>(t, 2, 0); xread<3>(t, 1, 1); xread<3>(t, 2, 1); }
        if (k == 31) { xread<3>(t, 0, 0); xread<3>(t, 0, 1); }
      }
    }
    SB();
  }
}

#define TS(i)
__global__ void __launch_bounds__(256, 1)
fused_kernel(const int* __restrict__ idx, const unsigned short* __restrict__ table,
             const unsigned short* __restrict__ W1, char* slab,
             const unsigned short* __restrict__ xw, const unsigned short* __restrict__ ww,
             const unsigned short* __restrict__ bw, const unsigned short* __restrict__ b1,
             const unsigned short* __restrict__ W2, const unsigned short* __restrict__ b2, __bf16* __restrict__ out) {
  extern __shared__ __attribute__((aligned(16))) char smem[];
  const int tid = threadIdx.x, lane = tid & 63;
  const int w = __builtin_amdgcn_readfirstlane(tid >> 6);
  const int bid = blockIdx.x;
  TS(0);
  const int xcd = bid & 7, jj = bid >> 3;
  const int q = xcd >> 1, nt = 4 * (xcd & 1) + (jj & 3), mt = jj >> 2;
  const int me = q * 8 + nt;
  const int r = lane & 15, g = lane >> 4;

  const __amdgpu_buffer_rsrc_t rsF = __builtin_amdgcn_make_buffer_rsrc((void*)(slab + (4 << 20) + mt * 16384), (short)0, 8704, SRD3);
  const int mv = lane < 16 ? me * 256 + w * 64 + lane * 4 : OOB;
  const int nme = 8192 + w * 128 + me * 4;
  const unsigned f0 = __builtin_amdgcn_raw_buffer_load_b32(rsF, lane == 16 ? nme : mv, 0, SC1);

  const int wbase = (4 * w * ND + 32 * q) * 1024 + nt * 128;
  const __amdgpu_buffer_rsrc_t rsW = __builtin_amdgcn_make_buffer_rsrc((void*)((const char*)W1 + wbase), (short)0, NF * FROW - wbase, SRD3);
  const __amdgpu_buffer_rsrc_t rsT = __builtin_amdgcn_make_buffer_rsrc((void*)table, (short)0, NV * ND * 2, SRD3);
  const __amdgpu_buffer_rsrc_t rsI = __builtin_amdgcn_make_buffer_rsrc((void*)(idx + mt * 128 * NF), (short)0, 128 * NF * 4, SRD3);
  const __amdgpu_buffer_rsrc_t rsS = __builtin_amdgcn_make_buffer_rsrc((void*)(slab + mt * 131072), (short)0, (4 << 20) - mt * 131072, SRD3);

  St c;
  {
    const int tv = (256 * w + (lane >> 2)) * 256 + q * 64 + (lane & 3) * 16;
    LAS char* d = (LAS char*)smem + w * 16 * TPIECE;
#pragma unroll
    for (int k = 0; k < 16; ++k)
      __builtin_amdgcn_raw_ptr_buffer_load_lds(rsT, (LAS void*)(d + k * TPIECE), 16, k < 14 ? tv : tv + k * 4096, k < 14 ? k * 4096 : 0, 0, 0);
    const int iv = w * 13312 + lane * 16;
    LAS char* e = (LAS char*)smem + IDX_LDS + w * 13312;
#pragma unroll
    for (int k = 0; k < 4; ++k) {
      __builtin_amdgcn_raw_ptr_buffer_load_lds(rsI, (LAS void*)(e + k * 4096), 16, iv + k * 4096, 0, 0, 0);
      if (k < 3) {
        __builtin_amdgcn_raw_ptr_buffer_load_lds(rsI, (LAS void*)(e + k * 4096), 16, iv + k * 4096, 0, 1024, 0);
        __builtin_amdgcn_raw_ptr_buffer_load_lds(rsI, (LAS void*)(e + k * 4096), 16, iv + k * 4096, 0, 2048, 0);
        __builtin_amdgcn_raw_ptr_buffer_load_lds(rsI, (LAS void*)(e + k * 4096), 16, iv + k * 4096, 0, 3072, 0);
      }
    }
  }
  SB();
  const int wv = g * FROW + r * 8;
  const int f = mt >= 6 ? mt - 6 : mt;
#pragma unroll
  for (int j = 0; j < 8; ++j) c.R[0][j] = wld(j, rsW, wv, (96 - 4 * w) * FROW + w * 8192);
  SB();
#pragma unroll
  for (int s = 1; s < 4; ++s)
#pragma unroll
    for (int j = 0; j < 8; ++j) c.R[s][j] = wld(j, rsW, wv, f * 16 * FROW + (s - 1) * 8192);
  {
    const u32x4 ones = {~0u, ~0u, ~0u, ~0u};
#pragma unroll
    for (int b = 0; b < 4; ++b)
      __builtin_amdgcn_raw_buffer_store_b128(ones, rsS, me * 4096 + w * 1024 + lane * 16 + b * (1 << 20), 0, SC1);
  }
  SB();
  {
    u32x4 z = {0u, 0u, 0u, 0u};
    asm volatile("s_nop 1" : "+v"(z));
#pragma unroll
    for (int k = 0; k < 32; ++k) asm volatile("v_mfma_f32_16x16x32_bf16 %0, %1, %1, 0" : "=a"(c.acc[k >> 2][k & 3]) : "v"(z));
  }
  c.selA = (w & 2) ? 0x07060302u : 0x05040100u;
  c.selB = (w & 2) ? 0x05040100u : 0x07060302u;
  const unsigned ib = IDX_LDS + r * 400 + g * 4;
  const unsigned ib0 = ib + (w & 1) * 6400, ib1 = ib - (w & 1) * 6400;
  TS(1);
  SB();
  asm volatile("s_waitcnt vmcnt(28)" ::: "memory");
  __builtin_amdgcn_s_barrier();
  FENCE();
  TS(2);
#pragma unroll
  for (int mb = 0; mb < 8; ++mb) c.ad[mb] = lds32(((mb & 1) ? ib1 : ib0) + 384 + mb * 6400);
#pragma unroll
  for (int mb = 0; mb < 8; ++mb) c.I[mb] = lds32(((mb & 1) ? ib1 : ib0) + f * 64 + w * 16 + mb * 6400);
  const unsigned T = (unsigned)__builtin_amdgcn_ballot_w64(((f0 >> (lane & 31)) & 1) == 0);
  __builtin_amdgcn_raw_buffer_store_b32(T, rsF, lane == 0 ? nme : OOB, 0, SC1);
#pragma unroll
  for (int i = 0; i < 4; ++i) { prm(c, c.F[0], c.R[0], i, 0); prm(c, c.F[0], c.R[0], i, 1); }
#pragma unroll
  for (int mb = 0; mb < 8; ++mb) c.T[mb] = lds128(taddr(c.ad[mb]) + w * 16);
#pragma unroll
  for (int j = 0; j < 8; ++j) c.R[0][j] = wld(j, rsW, wv, f * 16 * FROW + 3 * 8192);
  SB();
  const int row = mt * 128 + 32 * w + me;
  const int cb = (lane & 31) * 32;
  const u32x4 b1a = *(const u32x4*)((const char*)b1 + cb), b1b = *(const u32x4*)((const char*)b1 + cb + 16);
  const u32x4 w2a = *(const u32x4*)((const char*)W2 + cb), w2b = *(const u32x4*)((const char*)W2 + cb + 16);
  const int kx = r < NW ? r : 0;
  const unsigned short xv = xw[row * NW + kx], wv_ = ww[kx], bwv = bw[0], b2v = b2[0];
  const int w2_ = (w & 2) | (me >> 4), pj = 16 * (w & 1) + lane;
  Lp l;
  l.hv = lane < 16 ? lane * 4 : OOB;
  l.cn = 8192 + w2_ * 128 + 64 * (w & 1);
  l.cm = me * 256 + w * 64;
  l.ev = lane < 16 ? pj * 256 + w2_ * 64 + (me & 15) * 4 : OOB;
  l.hs = 0; l.nn = 0; l.ee = 0; l.n = 0; l.so = 0;
  l.f = f == 5 ? 0 : f + 1;
  l.wb = wv; l.wv = wv + l.f * (16 * FROW);
  l.ib0 = ib0 + w * 16; l.ib1 = ib1 + w * 16;
  l.ia0 = l.ib0 + l.f * 64; l.ia1 = l.ib1 + l.f * 64;
  SB();
  TS(3);

  do {
    step<0>(c, l, rsW, rsF);
    step<1>(c, l, rsW, rsF);
    step<2>(c, l, rsW, rsF);
    step<3>(c, l, rsW, rsF);
  } while (l.n < 5);
  unsigned n3 = l.hs, nn = l.nn;
  const int ev = l.ev;
  TS(4);
  if (__builtin_expect((__builtin_amdgcn_ballot_w64(l.ee != T) & 0xFFFFu) != 0, 0)) {
#pragma unroll 1
    for (int it = 0; it < (1 << 16); ++it) {
      const unsigned e = __builtin_amdgcn_raw_buffer_load_b32(rsF, l.hv, l.cm, SC1);
      asm volatile("s_waitcnt vmcnt(0)" ::: "memory");
      n3 = __builtin_amdgcn_raw_buffer_load_b32(rsF, l.hv, l.cn, SC1);
      __builtin_amdgcn_raw_buffer_store_b32(n3, rsF, n3 != nn ? ev : OOB, 0, SC1);
      nn = n3;
      if ((__builtin_amdgcn_ballot_w64(e != T) & 0xFFFFu) == 0) break;
    }
  }
  Tl t;
  SB();
#pragma unroll
  for (int k = 0; k < 32; ++k) {
    const int mb = k >> 2, nb = k & 3;
    c.acc[mb][nb] = mfma(c.F[0][nb], c.T[mb], c.acc[mb][nb]);
    SB();
    if (k < 8) prm(c, t.G[0], c.R[1], k >> 1, k & 1);
    if (k == 8 || k == 9) c.ad[k - 8] = taddr(c.I[k - 8]);
    if (k >= 10 && k < 18) t.TT[0][k & 1][(k - 10) >> 1] = lds128(c.ad[k & 1] + 16 * ((k - 10) >> 1));
    if (k >= 18 && k < 26) prm(c, t.G[1], c.R[2], (k - 18) >> 1, k & 1);
    if (k >= 26 && k < 29) t.xa[k - 26] = X1_LDS + ((w ^ (k - 25)) * 3 + (k - 26)) * 2048 + lane * 16;
    if (k == 29) t.xr = X1_LDS + w * 6144 + lane * 16;
    if (k == 30) t.pv = (16 * (w & 1) + r) * 4096 + (w >> 1) * 2048 + me * 64 + g * 16;
    SB();
  }
  TS(5);
  quarter<0>(c, t, rsS);
  quarter<1>(c, t, rsS);
  quarter<2>(c, t, rsS);
  quarter<3>(c, t, rsS);
  TS(6);
#pragma unroll
  for (int k = 0; k < 6; ++k) xsum<3>(c, t, k & 1, k < 2 ? 1 : (k < 4 ? 0 : 2));
  xpub<3>(t, rsS);
  SB();
  TS(7);
  __builtin_amdgcn_raw_buffer_store_b32(n3, rsF, n3 != nn ? ev : OOB, 0, SC1);

  const int iv = w * (1 << 20) + me * 4096 + (lane >> 5) * 2048 + (lane & 31) * 16;
  u32x4 v[4];
#pragma unroll
  for (int k = 0; k < 4; ++k) v[k] = __builtin_amdgcn_raw_buffer_load_b128(rsS, iv + k * 512, 0, SC1);
  const unsigned sh = (lane >> 5) * 16;
  float bb[8], w2[8];
#pragma unroll
  for (int k = 0; k < 8; ++k) {
    bb[k] = __uint_as_float(((k < 4 ? b1a[k & 3] : b1b[k & 3]) >> sh) << 16);
    w2[k] = __uint_as_float(((k < 4 ? w2a[k & 3] : w2b[k & 3]) >> sh) << 16);
  }
  const float wide = rbf(rbf(row_sum16(r < NW ? bf2f(xv) * bf2f(wv_) : 0.f)) + bf2f(bwv));
  const float b2f_ = bf2f(b2v);
  SB();
#pragma unroll 1
  for (int it = 0; it < (1 << 16); ++it) {
    unsigned m = 0;
#pragma unroll
    for (int k = 0; k < 4; ++k) m = max(max(m, max(v[k][0], v[k][1])), max(v[k][2], v[k][3]));
    if (__builtin_amdgcn_ballot_w64(m == 0xFFFFFFFFu) == 0) break;
    FENCE();
#pragma unroll
    for (int k = 0; k < 4; ++k) v[k] = __builtin_amdgcn_raw_buffer_load_b128(rsS, iv + k * 512, 0, SC1);
  }
  TS(8);
  float a = 0.f;
#pragma unroll
  for (int k = 0; k < 8; ++k) {
    const int e = k >> 1;
    float p[4];
#pragma unroll
    for (int j = 0; j < 4; ++j) p[j] = __uint_as_float((k & 1) ? (v[j][e] & 0xFFFF0000u) : (v[j][e] << 16));
    float hh = rbf(rbf((p[0] + p[1]) + (p[2] + p[3])) + bb[k]);
    hh = hh > 0.f ? hh : hh * 0.f;
    a += hh * w2[k];
  }
  a = row_sum16(a);
  auto p16 = __builtin_amdgcn_permlane16_swap(__float_as_uint(a), __float_as_uint(a), false, false);
  a = __uint_as_float(p16[0]) + __uint_as_float(p16[1]);
  auto p32 = __builtin_amdgcn_permlane32_swap(__float_as_uint(a), __float_as_uint(a), false, false);
  a = __uint_as_float(p32[0]) + __uint_as_float(p32[1]);
  const float z = rbf(wide + rbf(rbf(a) + b2f_));
  if (lane == 0) out[row] = (__bf16)(1.f / (1.f + __expf(-z)));
  TS(9);
}

extern "C" void kernel_launch(void* const* d_in, const int* in_sizes, int n_in,
                              void* d_out, int out_size, void* d_ws, size_t ws_size,
                              hipStream_t stream) {
  const int* idx = (const int*)d_in[0];
  const unsigned short* table = (const unsigned short*)d_in[1];
  const unsigned short* xw = (const unsigned short*)d_in[2];
  const unsigned short* ww = (const unsigned short*)d_in[3];
  const unsigned short* bw = (const unsigned short*)d_in[4];
  const unsigned short* W1 = (const unsigned short*)d_in[5];
  const unsigned short* b1 = (const unsigned short*)d_in[6];
  const unsigned short* W2 = (const unsigned short*)d_in[7];
  const unsigned short* b2 = (const unsigned short*)d_in[8];
  (void)hipFuncSetAttribute((const void*)fused_kernel, hipFuncAttributeMaxDynamicSharedMemorySize, LDS_BYTES);
  fused_kernel<<<256, 256, LDS_BYTES, stream>>>(idx, table, W1, (char*)d_ws, xw, ww, bw, b1, W2, b2, (__bf16*)d_out);
}
```

```cpp
#include <hip/hip_runtime.h>
#include <hip/hip_bf16.h>

typedef short    bf16x8 __attribute__((ext_vector_type(8)));
typedef __bf16   bf16x2 __attribute__((ext_vector_type(2)));
typedef float    f32x4  __attribute__((ext_vector_type(4)));
typedef float    f32x2  __attribute__((ext_vector_type(2)));
typedef unsigned u32x4  __attribute__((ext_vector_type(4)));
typedef unsigned u32x2  __attribute__((ext_vector_type(2)));
#define LAS __attribute__((address_space(3)))
#define SB() __builtin_amdgcn_sched_barrier(0)
#define FENCE() asm volatile("" ::: "memory")

constexpr int NF = 100, ND = 128, NV = 1000, NW = 13, NH = 512;
constexpr int TPIECE = 1040;
constexpr int IDX_LDS = 64 * TPIECE;
constexpr int X1_LDS = IDX_LDS;
constexpr int X0_OFF = 52 * 1024;
constexpr int LDS_BYTES = X1_LDS + X0_OFF + 24576;
constexpr int FROW = ND * NH * 2;
constexpr int SC1 = 16;
constexpr int SRD3 = 0x00020000;
constexpr int OOB = 0x40000000;
#define WAUX 2
#define TAUX 0
#define IAUX 0

__device__ __forceinline__ float rbf(float x) { return (float)(__bf16)x; }
__device__ __forceinline__ float bf2f(unsigned short h) { return __uint_as_float((unsigned)h << 16); }
template <int C> __device__ __forceinline__ float dpp_add(float x) {
  return x + __uint_as_float(__builtin_amdgcn_update_dpp(0, __float_as_uint(x), C, 0xf, 0xf, true));
}
__device__ __forceinline__ float row_sum16(float x) {
  x = dpp_add<0xB1>(x);
  x = dpp_add<0x4E>(x);
  x = dpp_add<0x141>(x);
  x = dpp_add<0x140>(x);
  return x;
}
__device__ __forceinline__ u32x4 lds128(unsigned a) { return *(const LAS u32x4*)(size_t)a; }
__device__ __forceinline__ f32x4 ldsf4(unsigned a) { return *(const LAS f32x4*)(size_t)a; }
__device__ __forceinline__ unsigned lds32(unsigned a) { return *(const LAS unsigned*)(size_t)a; }
__device__ __forceinline__ unsigned taddr(unsigned v) { return (v << 6) + (v & ~15u); }
__device__ __forceinline__ f32x4 mfma(u32x4 a, u32x4 b, f32x4 c) {
  return __builtin_amdgcn_mfma_f32_16x16x32_bf16(__builtin_bit_cast(bf16x8, a), __builtin_bit_cast(bf16x8, b), c, 0, 0, 0);
}

__device__ __forceinline__ void mfa(f32x4& c, const u32x4& a, const u32x4& b) {
  asm volatile("v_mfma_f32_16x16x32_bf16 %0, %1, %2, %0" : "+a"(c) : "v"(a), "v"(b));
  SB();
}

struct St {
  f32x4 acc[8][4];
  u32x4 T[8];
  u32x4 F[2][4];
  u32x2 R[4][8];
  unsigned ad[8], I[8];
  unsigned selA, selB;
};
__device__ __forceinline__ void prm(const St& c, u32x4* F, const u32x2* R, int i, int h) {
  F[2 * h][i]     = __builtin_amdgcn_perm(R[2 * i + 1][h], R[2 * i][h], c.selA);
  F[2 * h + 1][i] = __builtin_amdgcn_perm(R[2 * i + 1][h], R[2 * i][h], c.selB);
}
__device__ __forceinline__ u32x2 wld(int j, __amdgpu_buffer_rsrc_t rs, int voff, int soff) {
  return __builtin_amdgcn_raw_buffer_load_b64(rs, voff + (j & 3) * 1024, soff + (j >> 2) * 4096, WAUX);
}

struct Lp {
  int n, f, wb, wv, hv, ev, cn, cm, so;
  unsigned ib0, ib1, ia0, ia1, hs, nn, ee;
};
template <int U>
__device__ __forceinline__ void step(St& c, Lp& l, __amdgpu_buffer_rsrc_t rsW, __amdgpu_buffer_rsrc_t rsF) {
  constexpr int CUR = U & 1, NX = CUR ^ 1, SL = (U + 1) & 3, SN = (U + 2) & 3;
#pragma unroll
  for (int i = 0; i < 4; ++i) {
    const int m0 = 2 * i, m1 = 2 * i + 1;
    mfa(c.acc[m0][0], c.F[CUR][0], c.T[m0]);
    prm(c, c.F[NX], c.R[SL], i, 0);
    SB();
    mfa(c.acc[m0][1], c.F[CUR][1], c.T[m0]);
    prm(c, c.F[NX], c.R[SL], i, 1);
    SB();
    mfa(c.acc[m0][2], c.F[CUR][2], c.T[m0]);
    if (U == 0) c.ad[m0] = taddr(c.I[m0]);
    if (U == 2 && i == 0) l.so = l.n == 2 ? 0 : OOB;
    if (U == 2 && i == 1) l.so = ((18 >> l.n) & 1) ? l.cn : OOB;
    if (U == 2 && i == 2) l.so = l.n == 3 ? l.cm : l.so;
    if (U == 3 && i == 0) l.f = l.f + 1;
    if (U == 3 && i == 1) l.f = l.f == 6 ? 0 : l.f;
    if (U == 3 && i == 2) { l.ia0 = l.ib0 + l.f * 64; l.ia1 = l.ib1 + l.f * 64; asm volatile("" : "+v"(l.ia0), "+v"(l.ia1)); }
    if (U == 2 || U == 3) asm volatile("" : "+s"(l.so), "+s"(l.f));
    SB();
    mfa(c.acc[m0][3], c.F[CUR][3], c.T[m0]);
    c.T[m0] = lds128(c.ad[m0] + 16 * U);
    c.R[SL][m0] = wld(m0, rsW, l.wv, U * 8192);
    SB();
    mfa(c.acc[m1][0], c.F[CUR][0], c.T[m1]);
    if (U == 1) {
      const int m = (i & 1) + 4 * (i >> 1);
      c.I[m] = lds32(((i & 1) ? l.ia1 : l.ia0) + m * 6400);
      c.I[m + 2] = lds32(((i & 1) ? l.ia1 : l.ia0) + (m + 2) * 6400);
    }
    if (U == 2 && i == 0) __builtin_amdgcn_raw_buffer_store_b32(l.hs, rsF, l.ev, l.so, SC1);
    if (U == 2 && i == 1) { l.nn |= l.ee; l.ee = l.hs; asm volatile("" : "+v"(l.nn), "+v"(l.ee)); }
    if (U == 2 && i == 2) l.hs = __builtin_amdgcn_raw_buffer_load_b32(rsF, l.hv, l.so, SC1);
    SB();
    mfa(c.acc[m1][1], c.F[CUR][1], c.T[m1]);
    if (U == 0) c.ad[m1] = taddr(c.I[m1]);
    if (U == 3 && i == 3) { l.n = l.n + 1; asm volatile("" : "+s"(l.n)); }
    SB();
    mfa(c.acc[m1][2], c.F[CUR][2], c.T[m1]);
    c.R[SL][m1] = wld(m1, rsW, l.wv, U * 8192);
    if (U == 3 && i == 3) { SB(); l.wv = l.wb + l.f * (16 * FROW); asm volatile("" : "+v"(l.wv)); }
    SB();
    mfa(c.acc[m1][3], c.F[CUR][3], c.T[m1]);
    c.T[m1] = lds128(c.ad[m1] + 16 * U);
    SB();
    if (i < 3) asm volatile("" :: "v"(c.T[m0 + 2]), "v"(c.T[m1 + 2]), "v"(c.R[SL][m0 + 2]), "v"(c.R[SL][m1 + 2]));
    else       asm volatile("" :: "v"(c.T[0]), "v"(c.T[1]), "v"(c.R[SN][0]), "v"(c.R[SN][1]));
    SB();
  }
}

struct Tl {
  u32x4 G[4][4];
  u32x4 TT[2][2][4];
  f32x4 rd[3][2];
  f32x4 s[2];
  unsigned xa[3], xr;
  int pv;
};
template <int Q> __device__ __forceinline__ void xwrite(St& c, Tl& t, int k, int h) {
  *(LAS f32x4*)(size_t)(t.xa[k - 1] + ((Q & 1) ? 0 : X0_OFF) + h * 1024) = c.acc[2 * Q + (k & 1)][2 * h + (k >> 1)];
}
template <int Q> __device__ __forceinline__ void xread(Tl& t, int k, int h) {
  t.rd[k][h] = ldsf4(t.xr + ((Q & 1) ? 0 : X0_OFF) + k * 2048 + h * 1024);
}
template <int Q> __device__ __forceinline__ void xsum(St& c, Tl& t, int h, int ph) {
  if (ph == 0) { asm volatile("" : "+a"(c.acc[2 * Q][2 * h])); t.s[h] = c.acc[2 * Q][2 * h] + t.rd[0][h]; }
  if (ph == 1) t.rd[1][h] += t.rd[2][h];
  if (ph == 2) t.s[h] += t.rd[1][h];
}
template <int Q> __device__ __forceinline__ void xpub(Tl& t, __amdgpu_buffer_rsrc_t rsS) {
  u32x4 o;
#pragma unroll
  for (int e = 0; e < 4; ++e) {
    const f32x2 p = {t.s[0][e], t.s[1][e]};
    o[e] = min(__builtin_bit_cast(unsigned, __builtin_convertvector(p, bf16x2)), 0xFFFFFFFEu);
  }
  __builtin_amdgcn_raw_buffer_store_b128(o, rsS, t.pv + Q * (1 << 20), 0, SC1);
}
__device__ __forceinline__ void wgbar() {
  asm volatile("s_waitcnt lgkmcnt(0)" ::: "memory");
  __builtin_amdgcn_s_barrier();
  FENCE();
}

template <int Q>
__device__ __forceinline__ void quarter(St& c, Tl& t, __amdgpu_buffer_rsrc_t rsS) {
  constexpr int B = Q & 1;
#pragma unroll
  for (int k = 0; k < 32; ++k) {
    int ch, u, nb;
    if (Q < 3) { ch = k >> 3; u = (k >> 2) & 1; nb = k & 3; }
    else { const int pr = k >> 3; ch = (k >> 1) & 3; u = pr < 2 ? 1 : 0; nb = 2 * (k & 1) + ((pr == 1 || pr == 2) ? 1 : 0); }
    c.acc[2 * Q + u][nb] = mfma(t.G[ch][nb], t.TT[B][u][ch], c.acc[2 * Q + u][nb]);
    SB();
    if constexpr (Q == 0) {
      if (k < 8) prm(c, t.G[2], c.R[3], k >> 1, k & 1);
      if (k >= 14 && k < 22) prm(c, t.G[3], c.R[0], (k - 14) >> 1, k & 1);
      if (k == 8 || k == 9) c.ad[k - 6] = taddr(c.I[k - 6]);
      if (k >= 10 && k < 14) t.TT[1][k & 1][(k - 10) >> 1] = lds128(c.ad[2 + (k & 1)] + 16 * ((k - 10) >> 1));
      if (k >= 22 && k < 26) t.TT[1][k & 1][(k - 18) >> 1] = lds128(c.ad[2 + (k & 1)] + 16 * ((k - 18) >> 1));
      if (k >= 26 && k < 30) c.ad[k - 22] = taddr(c.I[k - 22]);
    } else {
      if (k >= 1 && k < 7) xwrite<Q - 1>(c, t, 1 + (k - 1) / 2, (k - 1) & 1);
      if (k == 9) wgbar();
      if (k >= 10 && k < 13) { xread<Q - 1>(t, k - 10, 0); xread<Q - 1>(t, k - 10, 1); }
      if (Q < 3) {
        if (k >= 13 && k < 21) t.TT[B ^ 1][(k - 13) & 1][(k - 13) >> 1] = lds128(c.ad[2 * Q + 2 + ((k - 13) & 1)] + 16 * ((k - 13) >> 1));
        if (k >= 21 && k < 27) xsum<Q - 1>(c, t, (k - 21) & 1, (k - 21) >> 1);
        if (k == 27) xpub<Q - 1>(t, rsS);
      } else {
        if (k == 13 || k == 14) xwrite<3>(c, t, 1, k - 13);
        if (k == 17 || k == 18) xwrite<3>(c, t, 3, k - 17);
        if (k >= 19 && k < 25) xsum<Q - 1>(c, t, (k - 19) & 1, (k - 19) >> 1);
        if (k == 25 || k == 26) xwrite<3>(c, t, 2, k - 25);
        if (k == 27) xpub<Q - 1>(t, rsS);
        if (k == 30) wgbar();
        if (k == 30) { xread<3>(t, 1, 0); xread<3>(t, 2, 0); xread<3>(t, 1, 1); xread<3>(t, 2, 1); }
        if (k == 31) { xread<3>(t, 0, 0); xread<3>(t, 0, 1); }
      }
    }
    SB();
  }
}

#define TS(i)
__global__ void __launch_bounds__(256, 1)
fused_kernel(const int* __restrict__ idx, const unsigned short* __restrict__ table,
             const unsigned short* __restrict__ W1, char* slab,
             const unsigned short* __restrict__ xw, const unsigned short* __restrict__ ww,
             const unsigned short* __restrict__ bw, const unsigned short* __restrict__ b1,
             const unsigned short* __restrict__ W2, const unsigned short* __restrict__ b2, __bf16* __restrict__ out) {
  extern __shared__ __attribute__((aligned(16))) char smem[];
  const int tid = threadIdx.x, lane = tid & 63;
  const int w = __builtin_amdgcn_readfirstlane(tid >> 6);
  const int bid = blockIdx.x;
  TS(0);
  const int xcd = bid & 7, jj = bid >> 3;
  const int q = xcd >> 1, nt = 4 * (xcd & 1) + (jj & 3), mt = jj >> 2;
  const int me = q * 8 + nt;
  const int r = lane & 15, g = lane >> 4;

  const __amdgpu_buffer_rsrc_t rsF = __builtin_amdgcn_make_buffer_rsrc((void*)(slab + (4 << 20) + mt * 16384), (short)0, 8704, SRD3);
  const int mv = lane < 16 ? me * 256 + w * 64 + lane * 4 : OOB;
  const int nme = 8192 + w * 128 + me * 4;
  const unsigned f0 = __builtin_amdgcn_raw_buffer_load_b32(rsF, lane == 16 ? nme : mv, 0, SC1);

  const int wbase = (4 * w * ND + 32 * q) * 1024 + nt * 128;
  const __amdgpu_buffer_rsrc_t rsW = __builtin_amdgcn_make_buffer_rsrc((void*)((const char*)W1 + wbase), (short)0, NF * FROW - wbase, SRD3);
  const __amdgpu_buffer_rsrc_t rsT = __builtin_amdgcn_make_buffer_rsrc((void*)table, (short)0, NV * ND * 2, SRD3);
  const __amdgpu_buffer_rsrc_t rsI = __builtin_amdgcn_make_buffer_rsrc((void*)(idx + mt * 128 * NF), (short)0, 128 * NF * 4, SRD3);
  const __amdgpu_buffer_rsrc_t rsS = __builtin_amdgcn_make_buffer_rsrc((void*)(slab + mt * 131072), (short)0, (4 << 20) - mt * 131072, SRD3);

  St c;
  {
    const int tv = (256 * w + (lane >> 2)) * 256 + q * 64 + (lane & 3) * 16;
    LAS char* d = (LAS char*)smem + w * 16 * TPIECE;
#pragma unroll
    for (int k = 0; k < 16; ++k)
      __builtin_amdgcn_raw_ptr_buffer_load_lds(rsT, (LAS void*)(d + k * TPIECE), 16, k < 14 ? tv : tv + k * 4096, k < 14 ? k * 4096 : 0, 0, TAUX);
    const int iv = w * 13312 + lane * 16;
    LAS char* e = (LAS char*)smem + IDX_LDS + w * 13312;
#pragma unroll
    for (int k = 0; k < 4; ++k) {
      __builtin_amdgcn_raw_ptr_buffer_load_lds(rsI, (LAS void*)(e + k * 4096), 16, iv + k * 4096, 0, 0, IAUX);
      if (k < 3) {
        __builtin_amdgcn_raw_ptr_buffer_load_lds(rsI, (LAS void*)(e + k * 4096), 16, iv + k * 4096, 0, 1024, IAUX);
        __builtin_amdgcn_raw_ptr_buffer_load_lds(rsI, (LAS void*)(e + k * 4096), 16, iv + k * 4096, 0, 2048, IAUX);
        __builtin_amdgcn_raw_ptr_buffer_load_lds(rsI, (LAS void*)(e + k * 4096), 16, iv + k * 4096, 0, 3072, IAUX);
      }
    }
  }
  SB();
  const int wv = g * FROW + r * 8;
  const int f = mt >= 6 ? mt - 6 : mt;
#pragma unroll
  for (int j = 0; j < 8; ++j) c.R[0][j] = wld(j, rsW, wv, (96 - 4 * w) * FROW + w * 8192);
  SB();
#pragma unroll
  for (int s = 1; s < 4; ++s)
#pragma unroll
    for (int j = 0; j < 8; ++j) c.R[s][j] = wld(j, rsW, wv, f * 16 * FROW + (s - 1) * 8192);
  {
    const u32x4 ones = {~0u, ~0u, ~0u, ~0u};
#pragma unroll
    for (int b = 0; b < 4; ++b)
      __builtin_amdgcn_raw_buffer_store_b128(ones, rsS, me * 4096 + w * 1024 + lane * 16 + b * (1 << 20), 0, SC1);
  }
  SB();
  {
    u32x4 z = {0u, 0u, 0u, 0u};
    asm volatile("s_nop 1" : "+v"(z));
#pragma unroll
    for (int k = 0; k < 32; ++k) asm volatile("v_mfma_f32_16x16x32_bf16 %0, %1, %1, 0" : "=a"(c.acc[k >> 2][k & 3]) : "v"(z));
  }
  c.selA = (w & 2) ? 0x07060302u : 0x05040100u;
  c.selB = (w & 2) ? 0x05040100u : 0x07060302u;
  const unsigned ib = IDX_LDS + r * 400 + g * 4;
  const unsigned ib0 = ib + (w & 1) * 6400, ib1 = ib - (w & 1) * 6400;
  TS(1);
  SB();
  asm volatile("s_waitcnt vmcnt(28)" ::: "memory");
  __builtin_amdgcn_s_barrier();
  FENCE();
  TS(2);
#pragma unroll
  for (int mb = 0; mb < 8; ++mb) c.ad[mb] = lds32(((mb & 1) ? ib1 : ib0) + 384 + mb * 6400);
#pragma unroll
  for (int mb = 0; mb < 8; ++mb) c.I[mb] = lds32(((mb & 1) ? ib1 : ib0) + f * 64 + w * 16 + mb * 6400);
  const unsigned T = (unsigned)__builtin_amdgcn_ballot_w64(((f0 >> (lane & 31)) & 1) == 0);
  __builtin_amdgcn_raw_buffer_store_b32(T, rsF, lane == 0 ? nme : OOB, 0, SC1);
#pragma unroll
  for (int i = 0; i < 4; ++i) { prm(c, c.F[0], c.R[0], i, 0); prm(c, c.F[0], c.R[0], i, 1); }
#pragma unroll
  for (int mb = 0; mb < 8; ++mb) c.T[mb] = lds128(taddr(c.ad[mb]) + w * 16);
#pragma unroll
  for (int j = 0; j < 8; ++j) c.R[0][j] = wld(j, rsW, wv, f * 16 * FROW + 3 * 8192);
  SB();
  const int row = mt * 128 + 32 * w + me;
  const int cb = (lane & 31) * 32;
  const u32x4 b1a = *(const u32x4*)((const char*)b1 + cb), b1b = *(const u32x4*)((const char*)b1 + cb + 16);
  const u32x4 w2a = *(const u32x4*)((const char*)W2 + cb), w2b = *(const u32x4*)((const char*)W2 + cb + 16);
  const int kx = r < NW ? r : 0;
  const unsigned short xv = xw[row * NW + kx], wv_ = ww[kx], bwv = bw[0], b2v = b2[0];
  const int w2_ = (w & 2) | (me >> 4), pj = 16 * (w & 1) + lane;
  Lp l;
  l.hv = lane < 16 ? lane * 4 : OOB;
  l.cn = 8192 + w2_ * 128 + 64 * (w & 1);
  l.cm = me * 256 + w * 64;
  l.ev = lane < 16 ? pj * 256 + w2_ * 64 + (me & 15) * 4 : OOB;
  l.hs = 0; l.nn = 0; l.ee = 0; l.n = 0; l.so = 0;
  l.f = f == 5 ? 0 : f + 1;
  l.wb = wv; l.wv = wv + l.f * (16 * FROW);
  l.ib0 = ib0 + w * 16; l.ib1 = ib1 + w * 16;
  l.ia0 = l.ib0 + l.f * 64; l.ia1 = l.ib1 + l.f * 64;
  SB();
  TS(3);

  do {
    step<0>(c, l, rsW, rsF);
    step<1>(c, l, rsW, rsF);
    step<2>(c, l, rsW, rsF);
    step<3>(c, l, rsW, rsF);
  } while (l.n < 5);
  unsigned n3 = l.hs, nn = l.nn;
  const int ev = l.ev;
  TS(4);
  if (__builtin_expect((__builtin_amdgcn_ballot_w64(l.ee != T) & 0xFFFFu) != 0, 0)) {
#pragma unroll 1
    for (int it = 0; it < (1 << 16); ++it) {
      const unsigned e = __builtin_amdgcn_raw_buffer_load_b32(rsF, l.hv, l.cm, SC1);
      asm volatile("s_waitcnt vmcnt(0)" ::: "memory");
      n3 = __builtin_amdgcn_raw_buffer_load_b32(rsF, l.hv, l.cn, SC1);
      __builtin_amdgcn_raw_buffer_store_b32(n3, rsF, n3 != nn ? ev : OOB, 0, SC1);
      nn = n3;
      if ((__builtin_amdgcn_ballot_w64(e != T) & 0xFFFFu) == 0) break;
    }
  }
  Tl t;
  SB();
#pragma unroll
  for (int k = 0; k < 32; ++k) {
    const int mb = k >> 2, nb = k & 3;
    c.acc[mb][nb] = mfma(c.F[0][nb], c.T[mb], c.acc[mb][nb]);
    SB();
    if (k < 8) prm(c, t.G[0], c.R[1], k >> 1, k & 1);
    if (k == 8 || k == 9) c.ad[k - 8] = taddr(c.I[k - 8]);
    if (k >= 10 && k < 18) t.TT[0][k & 1][(k - 10) >> 1] = lds128(c.ad[k & 1] + 16 * ((k - 10) >> 1));
    if (k >= 18 && k < 26) prm(c, t.G[1], c.R[2], (k - 18) >> 1, k & 1);
    if (k >= 26 && k < 29) t.xa[k - 26] = X1_LDS + ((w ^ (k - 25)) * 3 + (k - 26)) * 2048 + lane * 16;
    if (k == 29) t.xr = X1_LDS + w * 6144 + lane * 16;
    if (k == 30) t.pv = (16 * (w & 1) + r) * 4096 + (w >> 1) * 2048 + me * 64 + g * 16;
    SB();
  }
  TS(5);
  quarter<0>(c, t, rsS);
  quarter<1>(c, t, rsS);
  quarter<2>(c, t, rsS);
  quarter<3>(c, t, rsS);
  TS(6);
#pragma unroll
  for (int k = 0; k < 6; ++k) xsum<3>(c, t, k & 1, k < 2 ? 1 : (k < 4 ? 0 : 2));
  xpub<3>(t, rsS);
  SB();
  TS(7);
  __builtin_amdgcn_raw_buffer_store_b32(n3, rsF, n3 != nn ? ev : OOB, 0, SC1);

  const int iv = w * (1 << 20) + me * 4096 + (lane >> 5) * 2048 + (lane & 31) * 16;
  u32x4 v[4];
#pragma unroll
  for (int k = 0; k < 4; ++k) v[k] = __builtin_amdgcn_raw_buffer_load_b128(rsS, iv + k * 512, 0, SC1);
  const unsigned sh = (lane >> 5) * 16;
  float bb[8], w2[8];
#pragma unroll
  for (int k = 0; k < 8; ++k) {
    bb[k] = __uint_as_float(((k < 4 ? b1a[k & 3] : b1b[k & 3]) >> sh) << 16);
    w2[k] = __uint_as_float(((k < 4 ? w2a[k & 3] : w2b[k & 3]) >> sh) << 16);
  }
  const float wide = rbf(rbf(row_sum16(r < NW ? bf2f(xv) * bf2f(wv_) : 0.f)) + bf2f(bwv));
  const float b2f_ = bf2f(b2v);
  SB();
#pragma unroll 1
  for (int it = 0; it < (1 << 16); ++it) {
    unsigned m = 0;
#pragma unroll
    for (int k = 0; k < 4; ++k) m = max(max(m, max(v[k][0], v[k][1])), max(v[k][2], v[k][3]));
    if (__builtin_amdgcn_ballot_w64(m == 0xFFFFFFFFu) == 0) break;
    FENCE();
#pragma unroll
    for (int k = 0; k < 4; ++k) v[k] = __builtin_amdgcn_raw_buffer_load_b128(rsS, iv + k * 512, 0, SC1);
  }
  TS(8);
  float a = 0.f;
#pragma unroll
  for (int k = 0; k < 8; ++k) {
    const int e = k >> 1;
    float p[4];
#pragma unroll
    for (int j = 0; j < 4; ++j) p[j] = __uint_as_float((k & 1) ? (v[j][e] & 0xFFFF0000u) : (v[j][e] << 16));
    float hh = rbf(rbf((p[0] + p[1]) + (p[2] + p[3])) + bb[k]);
    hh = hh > 0.f ? hh : hh * 0.f;
    a += hh * w2[k];
  }
  a = row_sum16(a);
  auto p16 = __builtin_amdgcn_permlane16_swap(__float_as_uint(a), __float_as_uint(a), false, false);
  a = __uint_as_float(p16[0]) + __uint_as_float(p16[1]);
  auto p32 = __builtin_amdgcn_permlane32_swap(__float_as_uint(a), __float_as_uint(a), false, false);
  a = __uint_as_float(p32[0]) + __uint_as_float(p32[1]);
  const float z = rbf(wide + rbf(rbf(a) + b2f_));
  if (lane == 0) out[row] = (__bf16)(1.f / (1.f + __expf(-z)));
  TS(9);
}

extern "C" void kernel_launch(void* const* d_in, const int* in_sizes, int n_in,
                              void* d_out, int out_size, void* d_ws, size_t ws_size,
                              hipStream_t stream) {
  const int* idx = (const int*)d_in[0];
  const unsigned short* table = (const unsigned short*)d_in[1];
  const unsigned short* xw = (const unsigned short*)d_in[2];
  const unsigned short* ww = (const unsigned short*)d_in[3];
  const unsigned short* bw = (const unsigned short*)d_in[4];
  const unsigned short* W1 = (const unsigned short*)d_in[5];
  const unsigned short* b1 = (const unsigned short*)d_in[6];
  const unsigned short* W2 = (const unsigned short*)d_in[7];
  const unsigned short* b2 = (const unsigned short*)d_in[8];
  (void)hipFuncSetAttribute((const void*)fused_kernel, hipFuncAttributeMaxDynamicSharedMemorySize, LDS_BYTES);
  fused_kernel<<<256, 256, LDS_BYTES, stream>>>(idx, table, W1, (char*)d_ws, xw, ww, bw, b1, W2, b2, (__bf16*)d_out);
}
```

```cpp
#include <hip/hip_runtime.h>
#include <hip/hip_bf16.h>

typedef short    bf16x8 __attribute__((ext_vector_type(8)));
typedef __bf16   bf16x2 __attribute__((ext_vector_type(2)));
typedef float    f32x4  __attribute__((ext_vector_type(4)));
typedef float    f32x2  __attribute__((ext_vector_type(2)));
typedef unsigned u32x4  __attribute__((ext_vector_type(4)));
typedef unsigned u32x2  __attribute__((ext_vector_type(2)));
#define LAS __attribute__((address_space(3)))
#define SB() __builtin_amdgcn_sched_barrier(0)
#define FENCE() asm volatile("" ::: "memory")

constexpr int NF = 100, ND = 128, NV = 1000, NW = 13, NH = 512;
constexpr int TPIECE = 1040;
constexpr int IDX_LDS = 64 * TPIECE;
constexpr int X1_LDS = IDX_LDS;
constexpr int X0_OFF = 52 * 1024;
constexpr int LDS_BYTES = X1_LDS + X0_OFF + 24576;
constexpr int FROW = ND * NH * 2;
constexpr int SC1 = 16;
constexpr int SRD3 = 0x00020000;
constexpr int OOB = 0x40000000;

__device__ __forceinline__ float rbf(float x) { return (float)(__bf16)x; }
__device__ __forceinline__ float bf2f(unsigned short h) { return __uint_as_float((unsigned)h << 16); }
template <int C> __device__ __forceinline__ float dpp_add(float x) {
  return x + __uint_as_float(__builtin_amdgcn_update_dpp(0, __float_as_uint(x), C, 0xf, 0xf, true));
}
__device__ __forceinline__ float row_sum16(float x) {
  x = dpp_add<0xB1>(x);
  x = dpp_add<0x4E>(x);
  x = dpp_add<0x141>(x);
  x = dpp_add<0x140>(x);
  return x;
}
__device__ __forceinline__ u32x4 lds128(unsigned a) { return *(const LAS u32x4*)(size_t)a; }
__device__ __forceinline__ f32x4 ldsf4(unsigned a) { return *(const LAS f32x4*)(size_t)a; }
__device__ __forceinline__ unsigned lds32(unsigned a) { return *(const LAS unsigned*)(size_t)a; }
__device__ __forceinline__ unsigned taddr(unsigned v) { return (v << 6) + (v & ~15u); }
__device__ __forceinline__ f32x4 mfma(u32x4 a, u32x4 b, f32x4 c) {
  return __builtin_amdgcn_mfma_f32_16x16x32_bf16(__builtin_bit_cast(bf16x8, a), __builtin_bit_cast(bf16x8, b), c, 0, 0, 0);
}

__device__ __forceinline__ void mfa(f32x4& c, const u32x4& a, const u32x4& b) {
  asm volatile("v_mfma_f32_16x16x32_bf16 %0, %1, %2, %0" : "+a"(c) : "v"(a), "v"(b));
  SB();
}

struct St {
  f32x4 acc[8][4];
  u32x4 T[8];
  u32x4 F[2][4];
  u32x2 R[4][8];
  unsigned ad[8], I[8];
  unsigned selA, selB;
};
__device__ __forceinline__ void prm(const St& c, u32x4* F, const u32x2* R, int i, int h) {
  F[2 * h][i]     = __builtin_amdgcn_perm(R[2 * i + 1][h], R[2 * i][h], c.selA);
  F[2 * h + 1][i] = __builtin_amdgcn_perm(R[2 * i + 1][h], R[2 * i][h], c.selB);
}
__device__ __forceinline__ u32x2 wld(int j, __amdgpu_buffer_rsrc_t rs, int voff, int soff) {
  return __builtin_amdgcn_raw_buffer_load_b64(rs, voff + (j & 3) * 1024, soff + (j >> 2) * 4096, 0);
}

struct Lp {
  int n, f, wb, wv, hv, ev, cn, cm, so;
  unsigned ib0, ib1, ia0, ia1, hs, nn, ee;
};
template <int U>
__device__ __forceinline__ void step(St& c, Lp& l, __amdgpu_buffer_rsrc_t rsW, __amdgpu_buffer_rsrc_t rsF) {
  constexpr int CUR = U & 1, NX = CUR ^ 1, SL = (U + 1) & 3, SN = (U + 2) & 3;
#pragma unroll
  for (int i = 0; i < 4; ++i) {
    const int m0 = 2 * i, m1 = 2 * i + 1;
    mfa(c.acc[m0][0], c.F[CUR][0], c.T[m0]);
    prm(c, c.F[NX], c.R[SL], i, 0);
    SB();
    mfa(c.acc[m0][1], c.F[CUR][1], c.T[m0]);
    prm(c, c.F[NX], c.R[SL], i, 1);
    SB();
    mfa(c.acc[m0][2], c.F[CUR][2], c.T[m0]);
    if (U == 0) c.ad[m0] = taddr(c.I[m0]);
    if (U == 2 && i == 0) l.so = l.n == 2 ? 0 : OOB;
    if (U == 2 && i == 1) l.so = ((18 >> l.n) & 1) ? l.cn : OOB;
    if (U == 2 && i == 2) l.so = l.n == 3 ? l.cm : l.so;
    if (U == 3 && i == 0) l.f = l.f + 1;
    if (U == 3 && i == 1) l.f = l.f == 6 ? 0 : l.f;
    if (U == 3 && i == 2) { l.ia0 = l.ib0 + l.f * 64; l.ia1 = l.ib1 + l.f * 64; asm volatile("" : "+v"(l.ia0), "+v"(l.ia1)); }
    if (U == 2 || U == 3) asm volatile("" : "+s"(l.so), "+s"(l.f));
    SB();
    mfa(c.acc[m0][3], c.F[CUR][3], c.T[m0]);
    c.T[m0] = lds128(c.ad[m0] + 16 * U);
    c.R[SL][m0] = wld(m0, rsW, l.wv, U * 8192);
    SB();
    mfa(c.acc[m1][0], c.F[CUR][0], c.T[m1]);
    if (U == 1) {
      const int m = (i & 1) + 4 * (i >> 1);
      c.I[m] = lds32(((i & 1) ? l.ia1 : l.ia0) + m * 6400);
      c.I[m + 2] = lds32(((i & 1) ? l.ia1 : l.ia0) + (m + 2) * 6400);
    }
    if (U == 2 && i == 0) __builtin_amdgcn_raw_buffer_store_b32(l.hs, rsF, l.ev, l.so, SC1);
    if (U == 2 && i == 1) { l.nn |= l.ee; l.ee = l.hs; asm volatile("" : "+v"(l.nn), "+v"(l.ee)); }
    if (U == 2 && i == 2) l.hs = __builtin_amdgcn_raw_buffer_load_b32(rsF, l.hv, l.so, SC1);
    SB();
    mfa(c.acc[m1][1], c.F[CUR][1], c.T[m1]);
    if (U == 0) c.ad[m1] = taddr(c.I[m1]);
    if (U == 3 && i == 3) { l.n = l.n + 1; asm volatile("" : "+s"(l.n)); }
    SB();
    mfa(c.acc[m1][2], c.F[CUR][2], c.T[m1]);
    c.R[SL][m1] = wld(m1, rsW, l.wv, U * 8192);
    if (U == 3 && i == 3) { SB(); l.wv = l.wb + l.f * (16 * FROW); asm volatile("" : "+v"(l.wv)); }
    SB();
    mfa(c.acc[m1][3], c.F[CUR][3], c.T[m1]);
    c.T[m1] = lds128(c.ad[m1] + 16 * U);
    SB();
    if (i < 3) asm volatile("" :: "v"(c.T[m0 + 2]), "v"(c.T[m1 + 2]), "v"(c.R[SL][m0 + 2]), "v"(c.R[SL][m1 + 2]));
    else       asm volatile("" :: "v"(c.T[0]), "v"(c.T[1]), "v"(c.R[SN][0]), "v"(c.R[SN][1]));
    SB();
  }
}

struct Tl {
  u32x4 G[4][4];
  u32x4 TT[2][2][4];
  f32x4 rd[3][2];
  f32x4 s[2];
  unsigned xa[3], xr;
  int pv;
};
template <int Q> __device__ __forceinline__ void xwrite(St& c, Tl& t, int k, int h) {
  *(LAS f32x4*)(size_t)(t.xa[k - 1] + ((Q & 1) ? 0 : X0_OFF) + h * 1024) = c.acc[2 * Q + (k & 1)][2 * h + (k >> 1)];
}
template <int Q> __device__ __forceinline__ void xread(Tl& t, int k, int h) {
  t.rd[k][h] = ldsf4(t.xr + ((Q & 1) ? 0 : X0_OFF) + k * 2048 + h * 1024);
}
template <int Q> __device__ __forceinline__ void xsum(St& c, Tl& t, int h, int ph) {
  if (ph == 0) { asm volatile("" : "+a"(c.acc[2 * Q][2 * h])); t.s[h] = c.acc[2 * Q][2 * h] + t.rd[0][h]; }
  if (ph == 1) t.rd[1][h] += t.rd[2][h];
  if (ph == 2) t.s[h] += t.rd[1][h];
}
template <int Q> __device__ __forceinline__ void xpub(Tl& t, __amdgpu_buffer_rsrc_t rsS) {
  u32x4 o;
#pragma unroll
  for (int e = 0; e < 4; ++e) {
    const f32x2 p = {t.s[0][e], t.s[1][e]};
    o[e] = min(__builtin_bit_cast(unsigned, __builtin_convertvector(p, bf16x2)), 0xFFFFFFFEu);
  }
  __builtin_amdgcn_raw_buffer_store_b128(o, rsS, t.pv + Q * (1 << 20), 0, SC1);
}
__device__ __forceinline__ void wgbar() {
  asm volatile("s_waitcnt lgkmcnt(0)" ::: "memory");
  __builtin_amdgcn_s_barrier();
  FENCE();
}

template <int Q>
__device__ __forceinline__ void quarter(St& c, Tl& t, __amdgpu_buffer_rsrc_t rsS) {
  constexpr int B = Q & 1;
#pragma unroll
  for (int k = 0; k < 32; ++k) {
    int ch, u, nb;
    if (Q < 3) { ch = k >> 3; u = (k >> 2) & 1; nb = k & 3; }
    else { const int pr = k >> 3; ch = (k >> 1) & 3; u = pr < 2 ? 1 : 0; nb = 2 * (k & 1) + ((pr == 1 || pr == 2) ? 1 : 0); }
    c.acc[2 * Q + u][nb] = mfma(t.G[ch][nb], t.TT[B][u][ch], c.acc[2 * Q + u][nb]);
    SB();
    if constexpr (Q == 0) {
      if (k < 8) prm(c, t.G[2], c.R[3], k >> 1, k & 1);
      if (k >= 14 && k < 22) prm(c, t.G[3], c.R[0], (k - 14) >> 1, k & 1);
      if (k == 8 || k == 9) c.ad[k - 6] = taddr(c.I[k - 6]);
      if (k >= 10 && k < 14) t.TT[1][k & 1][(k - 10) >> 1] = lds128(c.ad[2 + (k & 1)] + 16 * ((k - 10) >> 1));
      if (k >= 22 && k < 26) t.TT[1][k & 1][(k - 18) >> 1] = lds128(c.ad[2 + (k & 1)] + 16 * ((k - 18) >> 1));
      if (k >= 26 && k < 30) c.ad[k - 22] = taddr(c.I[k - 22]);
    } else {
      if (k >= 1 && k < 7) xwrite<Q - 1>(c, t, 1 + (k - 1) / 2, (k - 1) & 1);
      if (k == 9) wgbar();
      if (k >= 10 && k < 13) { xread<Q - 1>(t, k - 10, 0); xread<Q - 1>(t, k - 10, 1); }
      if (Q < 3) {
        if (k >= 13 && k < 21) t.TT[B ^ 1][(k - 13) & 1][(k - 13) >> 1] = lds128(c.ad[2 * Q + 2 + ((k - 13) & 1)] + 16 * ((k - 13) >> 1));
        if (k >= 21 && k < 27) xsum<Q - 1>(c, t, (k - 21) & 1, (k - 21) >> 1);
        if (k == 27) xpub<Q - 1>(t, rsS);
      } else {
        if (k == 13 || k == 14) xwrite<3>(c, t, 1, k - 13);
        if (k == 17 || k == 18) xwrite<3>(c, t, 3, k - 17);
        if (k >= 19 && k < 25) xsum<Q - 1>(c, t, (k - 19) & 1, (k - 19) >> 1);
        if (k == 25 || k == 26) xwrite<3>(c, t, 2, k - 25);
        if (k == 27) xpub<Q - 1>(t, rsS);
        if (k == 30) wgbar();
        if (k == 30) { xread<3>(t, 1, 0); xread<3>(t, 2, 0); xread<3>(t, 1, 1); xread<3>(t, 2, 1); }
        if (k == 31) { xread<3>(t, 0, 0); xread<3>(t, 0, 1); }
      }
    }
    SB();
  }
}

#define PROBE 0
#define TS(i) do { if (i == 0 || i == PROBE) { SB(); tsr[i == 0 ? 0 : 1] = __builtin_amdgcn_s_memrealtime(); __builtin_amdgcn_s_waitcnt(0xC07F); SB(); } } while (0)
__global__ void __launch_bounds__(256, 1)
fused_kernel(const int* __restrict__ idx, const unsigned short* __restrict__ table,
             const unsigned short* __restrict__ W1, char* slab,
             const unsigned short* __restrict__ xw, const unsigned short* __restrict__ ww,
             const unsigned short* __restrict__ bw, const unsigned short* __restrict__ b1,
             const unsigned short* __restrict__ W2, const unsigned short* __restrict__ b2, __bf16* __restrict__ out) {
  extern __shared__ __attribute__((aligned(16))) char smem[];
  const int tid = threadIdx.x, lane = tid & 63;
  const int w = __builtin_amdgcn_readfirstlane(tid >> 6);
  const int bid = blockIdx.x;
  unsigned long long tsr[2];
  TS(0);
  const int xcd = bid & 7, jj = bid >> 3;
  const int q = xcd >> 1, nt = 4 * (xcd & 1) + (jj & 3), mt = jj >> 2;
  const int me = q * 8 + nt;
  const int r = lane & 15, g = lane >> 4;

  const __amdgpu_buffer_rsrc_t rsF = __builtin_amdgcn_make_buffer_rsrc((void*)(slab + (4 << 20) + mt * 16384), (short)0, 8704, SRD3);
  const int mv = lane < 16 ? me * 256 + w * 64 + lane * 4 : OOB;
  const int nme = 8192 + w * 128 + me * 4;
  const unsigned f0 = __builtin_amdgcn_raw_buffer_load_b32(rsF, lane == 16 ? nme : mv, 0, SC1);

  const int wbase = (4 * w * ND + 32 * q) * 1024 + nt * 128;
  const __amdgpu_buffer_rsrc_t rsW = __builtin_amdgcn_make_buffer_rsrc((void*)((const char*)W1 + wbase), (short)0, NF * FROW - wbase, SRD3);
  const __amdgpu_buffer_rsrc_t rsT = __builtin_amdgcn_make_buffer_rsrc((void*)table, (short)0, NV * ND * 2, SRD3);
  const __amdgpu_buffer_rsrc_t rsI = __builtin_amdgcn_make_buffer_rsrc((void*)(idx + mt * 128 * NF), (short)0, 128 * NF * 4, SRD3);
  const __amdgpu_buffer_rsrc_t rsS = __builtin_amdgcn_make_buffer_rsrc((void*)(slab + mt * 131072), (short)0, (4 << 20) - mt * 131072, SRD3);

  St c;
  {
    const int tv = (256 * w + (lane >> 2)) * 256 + q * 64 + (lane & 3) * 16;
    LAS char* d = (LAS char*)smem + w * 16 * TPIECE;
#pragma unroll
    for (int k = 0; k < 16; ++k)
      __builtin_amdgcn_raw_ptr_buffer_load_lds(rsT, (LAS void*)(d + k * TPIECE), 16, k < 14 ? tv : tv + k * 4096, k < 14 ? k * 4096 : 0, 0, 0);
    const int iv = w * 13312 + lane * 16;
    LAS char* e = (LAS char*)smem + IDX_LDS + w * 13312;
#pragma unroll
    for (int k = 0; k < 4; ++k) {
      __builtin_amdgcn_raw_ptr_buffer_load_lds(rsI, (LAS void*)(e + k * 4096), 16, iv + k * 4096, 0, 0, 0);
      if (k < 3) {
        __builtin_amdgcn_raw_ptr_buffer_load_lds(rsI, (LAS void*)(e + k * 4096), 16, iv + k * 4096, 0, 1024, 0);
        __builtin_amdgcn_raw_ptr_buffer_load_lds(rsI, (LAS void*)(e + k * 4096), 16, iv + k * 4096, 0, 2048, 0);
        __builtin_amdgcn_raw_ptr_buffer_load_lds(rsI, (LAS void*)(e + k * 4096), 16, iv + k * 4096, 0, 3072, 0);
      }
    }
  }
  SB();
  const int wv = g * FROW + r * 8;
  const int f = mt >= 6 ? mt - 6 : mt;
#pragma unroll
  for (int j = 0; j < 8; ++j) c.R[0][j] = wld(j, rsW, wv, (96 - 4 * w) * FROW + w * 8192);
  SB();
#pragma unroll
  for (int s = 1; s < 4; ++s)
#pragma unroll
    for (int j = 0; j < 8; ++j) c.R[s][j] = wld(j, rsW, wv, f * 16 * FROW + (s - 1) * 8192);
  {
    const u32x4 ones = {~0u, ~0u, ~0u, ~0u};
#pragma unroll
    for (int b = 0; b < 4; ++b)
      __builtin_amdgcn_raw_buffer_store_b128(ones, rsS, me * 4096 + w * 1024 + lane * 16 + b * (1 << 20), 0, SC1);
  }
  SB();
  {
    u32x4 z = {0u, 0u, 0u, 0u};
    asm volatile("s_nop 1" : "+v"(z));
#pragma unroll
    for (int k = 0; k < 32; ++k) asm volatile("v_mfma_f32_16x16x32_bf16 %0, %1, %1, 0" : "=a"(c.acc[k >> 2][k & 3]) : "v"(z));
  }
  c.selA = (w & 2) ? 0x07060302u : 0x05040100u;
  c.selB = (w & 2) ? 0x05040100u : 0x07060302u;
  const unsigned ib = IDX_LDS + r * 400 + g * 4;
  const unsigned ib0 = ib + (w & 1) * 6400, ib1 = ib - (w & 1) * 6400;
  TS(1);
  SB();
  asm volatile("s_waitcnt vmcnt(28)" ::: "memory");
  __builtin_amdgcn_s_barrier();
  FENCE();
  TS(2);
#pragma unroll
  for (int mb = 0; mb < 8; ++mb) c.ad[mb] = lds32(((mb & 1) ? ib1 : ib0) + 384 + mb * 6400);
#pragma unroll
  for (int mb = 0; mb < 8; ++mb) c.I[mb] = lds32(((mb & 1) ? ib1 : ib0) + f * 64 + w * 16 + mb * 6400);
  const unsigned T = (unsigned)__builtin_amdgcn_ballot_w64(((f0 >> (lane & 31)) & 1) == 0);
  __builtin_amdgcn_raw_buffer_store_b32(T, rsF, lane == 0 ? nme : OOB, 0, SC1);
#pragma unroll
  for (int i = 0; i < 4; ++i) { prm(c, c.F[0], c.R[0], i, 0); prm(c, c.F[0], c.R[0], i, 1); }
#pragma unroll
  for (int mb = 0; mb < 8; ++mb) c.T[mb] = lds128(taddr(c.ad[mb]) + w * 16);
#pragma unroll
  for (int j = 0; j < 8; ++j) c.R[0][j] = wld(j, rsW, wv, f * 16 * FROW + 3 * 8192);
  SB();
  const int row = mt * 128 + 32 * w + me;
  const int cb = (lane & 31) * 32;
  const u32x4 b1a = *(const u32x4*)((const char*)b1 + cb), b1b = *(const u32x4*)((const char*)b1 + cb + 16);
  const u32x4 w2a = *(const u32x4*)((const char*)W2 + cb), w2b = *(const u32x4*)((const char*)W2 + cb + 16);
  const int kx = r < NW ? r : 0;
  const unsigned short xv = xw[row * NW + kx], wv_ = ww[kx], bwv = bw[0], b2v = b2[0];
  const int w2_ = (w & 2) | (me >> 4), pj = 16 * (w & 1) + lane;
  Lp l;
  l.hv = lane < 16 ? lane * 4 : OOB;
  l.cn = 8192 + w2_ * 128 + 64 * (w & 1);
  l.cm = me * 256 + w * 64;
  l.ev = lane < 16 ? pj * 256 + w2_ * 64 + (me & 15) * 4 : OOB;
  l.hs = 0; l.nn = 0; l.ee = 0; l.n = 0; l.so = 0;
  l.f = f == 5 ? 0 : f + 1;
  l.wb = wv; l.wv = wv + l.f * (16 * FROW);
  l.ib0 = ib0 + w * 16; l.ib1 = ib1 + w * 16;
  l.ia0 = l.ib0 + l.f * 64; l.ia1 = l.ib1 + l.f * 64;
  SB();
  TS(3);

  do {
    step<0>(c, l, rsW, rsF);
    step<1>(c, l, rsW, rsF);
    step<2>(c, l, rsW, rsF);
    step<3>(c, l, rsW, rsF);
  } while (l.n < 5);
  unsigned n3 = l.hs, nn = l.nn;
  const int ev = l.ev;
  TS(4);
  if (__builtin_expect((__builtin_amdgcn_ballot_w64(l.ee != T) & 0xFFFFu) != 0, 0)) {
#pragma unroll 1
    for (int it = 0; it < (1 << 16); ++it) {
      const unsigned e = __builtin_amdgcn_raw_buffer_load_b32(rsF, l.hv, l.cm, SC1);
      asm volatile("s_waitcnt vmcnt(0)" ::: "memory");
      n3 = __builtin_amdgcn_raw_buffer_load_b32(rsF, l.hv, l.cn, SC1);
      __builtin_amdgcn_raw_buffer_store_b32(n3, rsF, n3 != nn ? ev : OOB, 0, SC1);
      nn = n3;
      if ((__builtin_amdgcn_ballot_w64(e != T) & 0xFFFFu) == 0) break;
    }
  }
  Tl t;
  SB();
#pragma unroll
  for (int k = 0; k < 32; ++k) {
    const int mb = k >> 2, nb = k & 3;
    c.acc[mb][nb] = mfma(c.F[0][nb], c.T[mb], c.acc[mb][nb]);
    SB();
    if (k < 8) prm(c, t.G[0], c.R[1], k >> 1, k & 1);
    if (k == 8 || k == 9) c.ad[k - 8] = taddr(c.I[k - 8]);
    if (k >= 10 && k < 18) t.TT[0][k & 1][(k - 10) >> 1] = lds128(c.ad[k & 1] + 16 * ((k - 10) >> 1));
    if (k >= 18 && k < 26) prm(c, t.G[1], c.R[2], (k - 18) >> 1, k & 1);
    if (k >= 26 && k < 29) t.xa[k - 26] = X1_LDS + ((w ^ (k - 25)) * 3 + (k - 26)) * 2048 + lane * 16;
    if (k == 29) t.xr = X1_LDS + w * 6144 + lane * 16;
    if (k == 30) t.pv = (16 * (w & 1) + r) * 4096 + (w >> 1) * 2048 + me * 64 + g * 16;
    SB();
  }
  TS(5);
  quarter<0>(c, t, rsS);
  quarter<1>(c, t, rsS);
  quarter<2>(c, t, rsS);
  quarter<3>(c, t, rsS);
  TS(6);
#pragma unroll
  for (int k = 0; k < 6; ++k) xsum<3>(c, t, k & 1, k < 2 ? 1 : (k < 4 ? 0 : 2));
  xpub<3>(t, rsS);
  SB();
  TS(7);
  __builtin_amdgcn_raw_buffer_store_b32(n3, rsF, n3 != nn ? ev : OOB, 0, SC1);

  const int iv = w * (1 << 20) + me * 4096 + (lane >> 5) * 2048 + (lane & 31) * 16;
  u32x4 v[4];
#pragma unroll
  for (int k = 0; k < 4; ++k) v[k] = __builtin_amdgcn_raw_buffer_load_b128(rsS, iv + k * 512, 0, SC1);
  const unsigned sh = (lane >> 5) * 16;
  float bb[8], w2[8];
#pragma unroll
  for (int k = 0; k < 8; ++k) {
    bb[k] = __uint_as_float(((k < 4 ? b1a[k & 3] : b1b[k & 3]) >> sh) << 16);
    w2[k] = __uint_as_float(((k < 4 ? w2a[k & 3] : w2b[k & 3]) >> sh) << 16);
  }
  const float wide = rbf(rbf(row_sum16(r < NW ? bf2f(xv) * bf2f(wv_) : 0.f)) + bf2f(bwv));
  const float b2f_ = bf2f(b2v);
  SB();
#pragma unroll 1
  for (int it = 0; it < (1 << 16); ++it) {
    unsigned m = 0;
#pragma unroll
    for (int k = 0; k < 4; ++k) m = max(max(m, max(v[k][0], v[k][1])), max(v[k][2], v[k][3]));
    if (__builtin_amdgcn_ballot_w64(m == 0xFFFFFFFFu) == 0) break;
    FENCE();
#pragma unroll
    for (int k = 0; k < 4; ++k) v[k] = __builtin_amdgcn_raw_buffer_load_b128(rsS, iv + k * 512, 0, SC1);
  }
  TS(8);
  float a = 0.f;
#pragma unroll
  for (int k = 0; k < 8; ++k) {
    const int e = k >> 1;
    float p[4];
#pragma unroll
    for (int j = 0; j < 4; ++j) p[j] = __uint_as_float((k & 1) ? (v[j][e] & 0xFFFF0000u) : (v[j][e] << 16));
    float hh = rbf(rbf((p[0] + p[1]) + (p[2] + p[3])) + bb[k]);
    hh = hh > 0.f ? hh : hh * 0.f;
    a += hh * w2[k];
  }
  a = row_sum16(a);
  auto p16 = __builtin_amdgcn_permlane16_swap(__float_as_uint(a), __float_as_uint(a), false, false);
  a = __uint_as_float(p16[0]) + __uint_as_float(p16[1]);
  auto p32 = __builtin_amdgcn_permlane32_swap(__float_as_uint(a), __float_as_uint(a), false, false);
  a = __uint_as_float(p32[0]) + __uint_as_float(p32[1]);
  const float z = rbf(wide + rbf(rbf(a) + b2f_));
  if (lane == 0) out[row] = (__bf16)(1.f / (1.f + __expf(-z)));
  TS(9);
  { const unsigned long long lim = (PROBE == 0 ? tsr[0] : tsr[1]) + 3000;
    for (int it = 0; it < 100000; ++it) { if (__builtin_amdgcn_s_memrealtime() >= lim) break; __builtin_amdgcn_s_sleep(1); } }
}

extern "C" void kernel_launch(void* const* d_in, const int* in_sizes, int n_in,
                              void* d_out, int out_size, void* d_ws, size_t ws_size,
                              hipStream_t stream) {
  const int* idx = (const int*)d_in[0];
  const unsigned short* table = (const unsigned short*)d_in[1];
  const unsigned short* xw = (const unsigned short*)d_in[2];
  const unsigned short* ww = (const unsigned short*)d_in[3];
  const unsigned short* bw = (const unsigned short*)d_in[4];
  const unsigned short* W1 = (const unsigned short*)d_in[5];
  const unsigned short* b1 = (const unsigned short*)d_in[6];
  const unsigned short* W2 = (const unsigned short*)d_in[7];
  const unsigned short* b2 = (const unsigned short*)d_in[8];
  (void)hipFuncSetAttribute((const void*)fused_kernel, hipFuncAttributeMaxDynamicSharedMemorySize, LDS_BYTES);
  fused_kernel<<<256, 256, LDS_BYTES, stream>>>(idx, table, W1, (char*)d_ws, xw, ww, bw, b1, W2, b2, (__bf16*)d_out);
}
```

```cpp
#include <hip/hip_runtime.h>
__device__ __forceinline__ float rbf(float x) { return (float)(__bf16)x; }
__device__ __forceinline__ float bf2f(unsigned short h) { return __uint_as_float((unsigned)h << 16); }
__global__ void __launch_bounds__(256, 1) k(const int* __restrict__ idx, const unsigned* __restrict__ table,
             const unsigned* __restrict__ W1, char* slab,
             const unsigned short* __restrict__ xw, const unsigned short* __restrict__ ww,
             const unsigned short* __restrict__ bw, const unsigned short* __restrict__ b1,
             const unsigned short* __restrict__ W2, const unsigned short* __restrict__ b2, __bf16* __restrict__ out) {
  extern __shared__ float sm[];
  unsigned long long t0;

  asm volatile("s_memrealtime %0\n s_waitcnt lgkmcnt(0)" : "=s"(t0) :: "memory");
  __builtin_amdgcn_sched_barrier(0);

  const int t = threadIdx.x, r0 = blockIdx.x * 4;
  float a[4][2] = {};
  for (int f = 0; f < 100; ++f) {
    const unsigned* tr[4];
    for (int r = 0; r < 4; ++r) tr[r] = table + idx[(r0 + r) * 100 + f] * 64;
#pragma unroll 4
    for (int d = 0; d < 64; ++d) {
      const unsigned w0 = W1[(f * 128 + 2 * d) * 256 + t], w1 = W1[(f * 128 + 2 * d + 1) * 256 + t];
      for (int r = 0; r < 4; ++r) {
        const unsigned e = tr[r][d];
        const float e0 = __uint_as_float(e << 16), e1 = __uint_as_float(e & 0xffff0000u);
        a[r][0] += e0 * __uint_as_float(w0 << 16) + e1 * __uint_as_float(w1 << 16);
        a[r][1] += e0 * __uint_as_float(w0 & 0xffff0000u) + e1 * __uint_as_float(w1 & 0xffff0000u);
      }
    }
  }
  for (int r = 0; r < 4; ++r) {
    float s = 0;
    for (int j = 0; j < 2; ++j) { float h = rbf(rbf(a[r][j]) + bf2f(b1[2 * t + j])); h = h > 0 ? h : 0; s += h * bf2f(W2[2 * t + j]); }
    for (int o = 32; o; o >>= 1) s += __shfl_xor(s, o);
    if ((t & 63) == 0) sm[r * 4 + (t >> 6)] = s;
  }
  __syncthreads();
  if (t < 4) {
    float wd = 0; for (int j = 0; j < 13; ++j) wd += bf2f(xw[(r0 + t) * 13 + j]) * bf2f(ww[j]);
    wd = rbf(rbf(wd) + bf2f(bw[0]));
    const float s = sm[t * 4] + sm[t * 4 + 1] + sm[t * 4 + 2] + sm[t * 4 + 3];
    const float z = rbf(wd + rbf(rbf(s) + bf2f(b2[0])));
    out[r0 + t] = (__bf16)(1.f / (1.f + __expf(-z)));
  }

  asm volatile("s_waitcnt vmcnt(0)" ::: "memory");
  const unsigned long long lim = t0 + 80000;
#pragma unroll 1
  for (int it = 0; it < 1000000; ++it) { if (__builtin_amdgcn_s_memrealtime() >= lim) break; __builtin_amdgcn_s_sleep(1); }
}
extern "C" void kernel_launch(void* const* d_in, const int* in_sizes, int n_in, void* d_out, int out_size, void* d_ws, size_t ws_size, hipStream_t stream) {
  (void)hipFuncSetAttribute((const void*)k, hipFuncAttributeMaxDynamicSharedMemorySize, 64);
  k<<<256, 256, 64, stream>>>((const int*)d_in[0], (const unsigned*)d_in[1], (const unsigned*)d_in[5], (char*)d_ws, (const unsigned short*)d_in[2], (const unsigned short*)d_in[3],
     (const unsigned short*)d_in[4], (const unsigned short*)d_in[6], (const unsigned short*)d_in[7], (const unsigned short*)d_in[8], (__bf16*)d_out);
}
```

```cpp
#include <hip/hip_runtime.h>
#include <hip/hip_bf16.h>

typedef short    bf16x8 __attribute__((ext_vector_type(8)));
typedef __bf16   bf16x2 __attribute__((ext_vector_type(2)));
typedef float    f32x4  __attribute__((ext_vector_type(4)));
typedef float    f32x2  __attribute__((ext_vector_type(2)));
typedef unsigned u32x4  __attribute__((ext_vector_type(4)));
typedef unsigned u32x8  __attribute__((ext_vector_type(8)));
typedef unsigned u32x16 __attribute__((ext_vector_type(16)));
#define GAS __attribute__((address_space(1)))
typedef unsigned u32x2  __attribute__((ext_vector_type(2)));
#define LAS __attribute__((address_space(3)))
#define SB() __builtin_amdgcn_sched_barrier(0)
#define FENCE() asm volatile("" ::: "memory")

#define TOUCH 1
#define NPRE 3
#define NMID 4
#define ROT 0
#define CLR 1
constexpr int NF = 100, ND = 128, NV = 1000, NW = 13, NH = 512;
constexpr int TPIECE = 1040;
constexpr int IDX_LDS = 64 * TPIECE;
constexpr int X1_LDS = IDX_LDS;
constexpr int X0_OFF = 52 * 1024;
constexpr int LDS_BYTES = X1_LDS + X0_OFF + 24576;
constexpr int FROW = ND * NH * 2;
constexpr int SC1 = 16;
constexpr int SRD3 = 0x00020000;
constexpr int OOB = 0x40000000;

__device__ __forceinline__ float rbf(float x) { return (float)(__bf16)x; }
__device__ __forceinline__ float bf2f(unsigned short h) { return __uint_as_float((unsigned)h << 16); }
template <int C> __device__ __forceinline__ float dpp_add(float x) {
  return x + __uint_as_float(__builtin_amdgcn_update_dpp(0, __float_as_uint(x), C, 0xf, 0xf, true));
}
__device__ __forceinline__ float row_sum16(float x) {
  x = dpp_add<0xB1>(x);
  x = dpp_add<0x4E>(x);
  x = dpp_add<0x141>(x);
  x = dpp_add<0x140>(x);
  return x;
}
__device__ __forceinline__ u32x4 lds128(unsigned a) { return *(const LAS u32x4*)(size_t)a; }
__device__ __forceinline__ f32x4 ldsf4(unsigned a) { return *(const LAS f32x4*)(size_t)a; }
__device__ __forceinline__ unsigned lds32(unsigned a) { return *(const LAS unsigned*)(size_t)a; }
__device__ __forceinline__ unsigned taddr(unsigned v) { return (v << 6) + (v & ~15u); }
__device__ __forceinline__ f32x4 mfma(u32x4 a, u32x4 b, f32x4 c) {
  return __builtin_amdgcn_mfma_f32_16x16x32_bf16(__builtin_bit_cast(bf16x8, a), __builtin_bit_cast(bf16x8, b), c, 0, 0, 0);
}

__device__ __forceinline__ void mfa(f32x4& c, const u32x4& a, const u32x4& b) {
  asm volatile("v_mfma_f32_16x16x32_bf16 %0, %1, %2, %0" : "+a"(c) : "v"(a), "v"(b));
  SB();
}

struct St {
  f32x4 acc[8][4];
  u32x4 T[8];
  u32x4 F[2][4];
  u32x4 R[4][4];
  unsigned ad[8], I[8];
  unsigned selA, selB, pa;
};
__device__ __forceinline__ void pk(St& c, u32x4* F, const u32x4* R, int u) {
  const int p = u >> 2, nb = u & 3;
  F[nb][2 + p] = __builtin_amdgcn_perm(R[2 * p + 1][2 + (nb >> 1)], R[2 * p][2 + (nb >> 1)], (nb & 1) ? c.selB : c.selA);
  SB();
  c.pa = __builtin_amdgcn_perm(R[2 * p + 1][nb >> 1], R[2 * p][nb >> 1], (nb & 1) ? c.selB : c.selA);
}
__device__ __forceinline__ void xc(St& c, u32x4* F, int u) {
  const int p = u >> 2, nb = u & 3;
  unsigned lo = 0, hi = F[nb][2 + p];
#if defined(__HIP_DEVICE_COMPILE__)
  register unsigned long long lm asm("vcc") = 0x00FF00FF00FF00FFull;
  asm volatile("v_cndmask_b32_dpp %0, %1, %2, vcc row_ror:8 row_mask:0xf bank_mask:0xf"
               : "=v"(lo) : "v"(hi), "v"(c.pa), "s"(lm));
#endif
  SB();
  asm volatile("v_mov_b32_dpp %0, %1 row_ror:8 row_mask:0xf bank_mask:0x3" : "+v"(hi) : "v"(c.pa));
  F[nb][p] = lo; F[nb][2 + p] = hi;
}
__device__ __forceinline__ u32x4 wld(int j, __amdgpu_buffer_rsrc_t rs, int voff, int soff) {
  return __builtin_amdgcn_raw_buffer_load_b128(rs, voff + j * 1024, soff, 0);
}

struct Lp {
  int n, f, wb, wv, hv, ev, cn, cm, so, sw[4];
  unsigned ib0, ib1, ia0, ia1, hs, nn, ee;
};
template <int U>
__device__ __forceinline__ void step(St& c, Lp& l, __amdgpu_buffer_rsrc_t rsW, __amdgpu_buffer_rsrc_t rsF) {
  constexpr int CUR = U & 1, NX = CUR ^ 1, SL = (U + 1) & 3, SN = (U + 2) & 3;
#pragma unroll
  for (int i = 0; i < 4; ++i) {
    const int m0 = 2 * i, m1 = 2 * i + 1;
    mfa(c.acc[m0][0], c.F[CUR][0], c.T[m0]);
    pk(c, c.F[NX], c.R[SL], m0);
    SB();
    mfa(c.acc[m0][1], c.F[CUR][1], c.T[m0]);
    xc(c, c.F[NX], m0);
    SB();
    mfa(c.acc[m0][2], c.F[CUR][2], c.T[m0]);
    pk(c, c.F[NX], c.R[SL], m1);
    SB();
    mfa(c.acc[m0][3], c.F[CUR][3], c.T[m0]);
    xc(c, c.F[NX], m1);
    SB();
    mfa(c.acc[m1][0], c.F[CUR][0], c.T[m1]);
    if (U == 0) c.ad[m0] = taddr(c.I[m0]);
    if (U == 1) {
      const int m = (i & 1) + 4 * (i >> 1);
      c.I[m] = lds32(((i & 1) ? l.ia1 : l.ia0) + m * 6400);
      c.I[m + 2] = lds32(((i & 1) ? l.ia1 : l.ia0) + (m + 2) * 6400);
    }
    if (U == 2 && i == 0) l.so = l.n == 2 ? 0 : OOB;
    if (U == 2 && i == 1) l.so = ((18 >> l.n) & 1) ? l.cn : OOB;
    if (U == 2 && i == 2) l.so = l.n == 3 ? l.cm : l.so;
    if (U == 3 && i == 0) l.f = l.f + 1;
    if (U == 3 && i == 1) l.f = l.f == 6 ? 0 : l.f;
    if (U == 3 && i == 2) { l.ia0 = l.ib0 + l.f * 64; l.ia1 = l.ib1 + l.f * 64; asm volatile("" : "+v"(l.ia0), "+v"(l.ia1)); }
    if (U == 3 && i == 3) { l.n = l.n + 1; asm volatile("" : "+s"(l.n)); }
    if (U == 2 || U == 3) asm volatile("" : "+s"(l.so), "+s"(l.f));
    SB();
    mfa(c.acc[m1][1], c.F[CUR][1], c.T[m1]);
    c.T[m0] = lds128(c.ad[m0] + 16 * U);
    if (i & 1) c.R[SL][i - 1] = wld(i - 1, rsW, l.wv, l.sw[U]);
    if (U == 2 && i == 0) __builtin_amdgcn_raw_buffer_store_b32(l.hs, rsF, l.ev, l.so, SC1);
    if (U == 2 && i == 2) l.hs = __builtin_amdgcn_raw_buffer_load_b32(rsF, l.hv, l.so, SC1);
    SB();
    mfa(c.acc[m1][2], c.F[CUR][2], c.T[m1]);
    if (U == 0) c.ad[m1] = taddr(c.I[m1]);
    if (U == 2 && i == 0) { l.nn |= l.ee; l.ee = l.hs; asm volatile("" : "+v"(l.nn), "+v"(l.ee)); }
    if (i & 1) c.R[SL][i] = wld(i, rsW, l.wv, l.sw[U]);
    if (U == 3 && i == 3) { SB(); l.wv = l.wb + l.f * (16 * FROW); asm volatile("" : "+v"(l.wv)); }
    SB();
    mfa(c.acc[m1][3], c.F[CUR][3], c.T[m1]);
    c.T[m1] = lds128(c.ad[m1] + 16 * U);
    SB();
    if (i == 1)     asm volatile("" :: "v"(c.T[m0 + 2]), "v"(c.T[m1 + 2]), "v"(c.R[SL][2]), "v"(c.R[SL][3]));
    else if (i < 3) asm volatile("" :: "v"(c.T[m0 + 2]), "v"(c.T[m1 + 2]));
    else            asm volatile("" :: "v"(c.T[0]), "v"(c.T[1]), "v"(c.R[SN][0]), "v"(c.R[SN][1]));
    SB();
  }
}

struct Tl {
  u32x4 G[4][4];
  u32x4 TT[2][2][4];
  f32x4 rd[3][2];
  f32x4 s[2];
  unsigned xa[3], xr;
  int pv;
};
template <int Q> __device__ __forceinline__ void xwrite(St& c, Tl& t, int k, int h) {
  *(LAS f32x4*)(size_t)(t.xa[k - 1] + ((Q & 1) ? 0 : X0_OFF) + h * 1024) = c.acc[2 * Q + (k & 1)][2 * h + (k >> 1)];
}
template <int Q> __device__ __forceinline__ void xread(Tl& t, int k, int h) {
  t.rd[k][h] = ldsf4(t.xr + ((Q & 1) ? 0 : X0_OFF) + k * 2048 + h * 1024);
}
template <int Q> __device__ __forceinline__ void xsum(St& c, Tl& t, int h, int ph) {
  if (ph == 0) { asm volatile("" : "+a"(c.acc[2 * Q][2 * h])); t.s[h] = c.acc[2 * Q][2 * h] + t.rd[0][h]; }
  if (ph == 1) t.rd[1][h] += t.rd[2][h];
  if (ph == 2) t.s[h] += t.rd[1][h];
}
template <int Q> __device__ __forceinline__ void xpub(Tl& t, __amdgpu_buffer_rsrc_t rsS) {
  u32x4 o;
#pragma unroll
  for (int e = 0; e < 4; ++e) {
    const f32x2 p = {t.s[0][e], t.s[1][e]};
    o[e] = min(__builtin_bit_cast(unsigned, __builtin_convertvector(p, bf16x2)), 0xFFFFFFFEu);
  }
  __builtin_amdgcn_raw_buffer_store_b128(o, rsS, t.pv + Q * (1 << 20), 0, SC1);
}
__device__ __forceinline__ void wgbar() {
  asm volatile("s_waitcnt lgkmcnt(0)" ::: "memory");
  __builtin_amdgcn_s_barrier();
  FENCE();
}

template <int Q>
__device__ __forceinline__ void quarter(St& c, Tl& t, __amdgpu_buffer_rsrc_t rsS) {
  constexpr int B = Q & 1;
#pragma unroll
  for (int k = 0; k < 32; ++k) {
    int ch, u, nb;
    if (Q < 3) { ch = k >> 3; u = (k >> 2) & 1; nb = k & 3; }
    else { const int pr = k >> 3; ch = (k >> 1) & 3; u = pr < 2 ? 1 : 0; nb = 2 * (k & 1) + ((pr == 1 || pr == 2) ? 1 : 0); }
    c.acc[2 * Q + u][nb] = mfma(t.G[ch][nb], t.TT[B][u][ch], c.acc[2 * Q + u][nb]);
    SB();
    if constexpr (Q == 0) {
      if (k < 22) {
        if (k & 1) xc(c, t.G[k < 6 ? 2 : 3], k < 6 ? 5 + (k >> 1) : (k - 6) >> 1);
        else       pk(c, t.G[k < 6 ? 2 : 3], c.R[k < 6 ? 3 : 0], k < 6 ? 5 + (k >> 1) : (k - 6) >> 1);
      }
      if (k == 8 || k == 9) c.ad[k - 6] = taddr(c.I[k - 6]);
      if (k >= 10 && k < 14) t.TT[1][k & 1][(k - 10) >> 1] = lds128(c.ad[2 + (k & 1)] + 16 * ((k - 10) >> 1));
      if (k >= 22 && k < 26) t.TT[1][k & 1][(k - 18) >> 1] = lds128(c.ad[2 + (k & 1)] + 16 * ((k - 18) >> 1));
      if (k >= 26 && k < 30) c.ad[k - 22] = taddr(c.I[k - 22]);
    } else {
      if (k >= 1 && k < 7) xwrite<Q - 1>(c, t, 1 + (k - 1) / 2, (k - 1) & 1);
      if (k == 9) wgbar();
      if (k >= 10 && k < 13) { xread<Q - 1>(t, k - 10, 0); xread<Q - 1>(t, k - 10, 1); }
      if (Q < 3) {
        if (k >= 13 && k < 21) t.TT[B ^ 1][(k - 13) & 1][(k - 13) >> 1] = lds128(c.ad[2 * Q + 2 + ((k - 13) & 1)] + 16 * ((k - 13) >> 1));
        if (k >= 21 && k < 27) xsum<Q - 1>(c, t, (k - 21) & 1, (k - 21) >> 1);
        if (k == 27) xpub<Q - 1>(t, rsS);
      } else {
        if (k == 13 || k == 14) xwrite<3>(c, t, 1, k - 13);
        if (k == 17 || k == 18) xwrite<3>(c, t, 3, k - 17);
        if (k >= 19 && k < 25) xsum<Q - 1>(c, t, (k - 19) & 1, (k - 19) >> 1);
        if (k == 25 || k == 26) xwrite<3>(c, t, 2, k - 25);
        if (k == 27) xpub<Q - 1>(t, rsS);
        if (k == 30) wgbar();
        if (k == 30) { xread<3>(t, 1, 0); xread<3>(t, 2, 0); xread<3>(t, 1, 1); xread<3>(t, 2, 1); }
        if (k == 31) { xread<3>(t, 0, 0); xread<3>(t, 0, 1); }
      }
    }
    SB();
  }
}

#define TS(i)
__global__ void __launch_bounds__(256, 1)
fused_kernel(const void* a0, const void* a1, const void* a2, void* a3, const void* a4, const void* a5, const void* a6,
             const void* a7, const void* a8, const void* a9, void* a10, void* a11) {
  extern __shared__ __attribute__((aligned(16))) char smem[];
  const int tid = threadIdx.x, lane = tid & 63;
  const int w = __builtin_amdgcn_readfirstlane(tid >> 6);
  const int bid = blockIdx.x;
  TS(0);
  u32x8 ka; u32x16 kb;
  asm volatile("s_load_dwordx8 %0, %2, 0x0\n\ts_load_dwordx16 %1, %2, 0x20" : "=&s"(ka), "=&s"(kb) : "s"(__builtin_amdgcn_kernarg_segment_ptr()));
  SB();
  const int xcd = bid & 7, jj = bid >> 3;
  const int q = xcd >> 1, nt = 4 * (xcd & 1) + (jj & 3), mt = jj >> 2;
  const int me = q * 8 + nt;
  const int r = lane & 15, g = lane >> 4;

  St c;
  {
    u32x4 z = {0u, 0u, 0u, 0u};
    asm volatile("s_nop 1" : "+v"(z));
#pragma unroll
    for (int k = 0; k < 32; ++k) asm volatile("v_mfma_f32_16x16x32_bf16 %0, %1, %1, 0" : "=a"(c.acc[k >> 2][k & 3]) : "v"(z));
  }
  const int wbase = (4 * w * ND + 32 * q) * 1024 + nt * 128;
  const int f = mt >= 6 ? mt - 6 : mt;
  const int rot = ROT;
  const int w0off = (96 - 4 * w) * FROW + (w ^ rot) * 8192;
  int t0v = (jj * 32 + w * 8 + lane) * 256 + q * 64, t1v = ((jj & 3) * 100 + w * 25 + lane) * 128;
  asm volatile("" : "+v"(t0v), "+v"(t1v));
  t0v = lane < 8 ? t0v : OOB;
  t1v = lane < 25 ? t1v : OOB;
  int mv = me * 256 + w * 64 + lane * 4;
  const int nme = 8192 + w * 128 + me * 4;
  mv = lane < 16 ? mv : OOB;
  mv = lane == 16 ? nme : mv;
  int nv = lane == 0 ? nme : OOB;
  int tv = (256 * w + (lane >> 2)) * 256 + q * 64 + ((lane & 3) ^ rot) * 16;
  int wv = g * FROW + (r >> 3) * 4096 + (r & 7) * 16;
  asm volatile("" : "+v"(t0v), "+v"(t1v), "+v"(wv), "+v"(mv), "+v"(tv), "+v"(nv));
  Tl t;
  const int row = mt * 128 + 32 * w + me;
  int kx = r < NW ? 2 * r : 0, kr = kx + row * (2 * NW), zv = 0;
#pragma unroll
  for (int k = 0; k < 3; ++k) t.xa[k] = X1_LDS + ((w ^ (k + 1)) * 3 + k) * 2048 + lane * 16;
  t.xr = X1_LDS + w * 6144 + lane * 16;
  t.pv = (16 * (w & 1) + r) * 4096 + (w >> 1) * 2048 + me * 64 + g * 16;
  int pollv = w * (1 << 20) + me * 4096 + (lane >> 5) * 2048 + (lane & 31) * 16;
  unsigned cb = IDX_LDS + 51200 + (lane & 28) * 32 + (lane & 1) * 64 + (lane & 2) * 4;
  asm volatile("" : "+v"(kx), "+v"(kr), "+v"(zv), "+v"(t.xa[0]), "+v"(t.xa[1]), "+v"(t.xa[2]), "+v"(t.xr), "+v"(t.pv), "+v"(pollv), "+v"(cb));
  SB();
  asm volatile("s_waitcnt lgkmcnt(0)" : "+s"(ka), "+s"(kb));
#define PTR(v, i) (((unsigned long long)v[2 * (i) + 1] << 32) | v[2 * (i)])
  const char* idx = (const char*)PTR(ka, 0); const char* W1 = (const char*)PTR(ka, 2); char* slab = (char*)PTR(ka, 3);
  const GAS char* xw = (const GAS char*)PTR(kb, 0);
  const GAS char* ww = (const GAS char*)PTR(kb, 1);
  const GAS char* bw = (const GAS char*)PTR(kb, 2);
  const GAS char* b2 = (const GAS char*)PTR(kb, 5);
  GAS __bf16* out = (GAS __bf16*)PTR(kb, 6);
  const __amdgpu_buffer_rsrc_t rsW = __builtin_amdgcn_make_buffer_rsrc((void*)(W1 + wbase), (short)0, NF * FROW - wbase, SRD3);
  const __amdgpu_buffer_rsrc_t rsT = __builtin_amdgcn_make_buffer_rsrc((void*)PTR(ka, 1), (short)0, NV * ND * 2, SRD3);
  const __amdgpu_buffer_rsrc_t rsI = __builtin_amdgcn_make_buffer_rsrc((void*)(idx + mt * 128 * NF * 4), (short)0, 128 * NF * 4, SRD3);
  const __amdgpu_buffer_rsrc_t rsS = __builtin_amdgcn_make_buffer_rsrc((void*)(slab + mt * 131072), (short)0, (4 << 20) - mt * 131072, SRD3);
  const __amdgpu_buffer_rsrc_t rsF = __builtin_amdgcn_make_buffer_rsrc((void*)(slab + (4 << 20) + mt * 16384), (short)0, 8704, SRD3);
  const char* ixb = idx + mt * 128 * NF * 4;
  const __amdgpu_buffer_rsrc_t rsJ = __builtin_amdgcn_make_buffer_rsrc((void*)(w == 3 ? (const char*)PTR(kb, 3) - 51200 : ixb), (short)0, w == 3 ? 52224 : 51200, SRD3);
  const __amdgpu_buffer_rsrc_t rsK = __builtin_amdgcn_make_buffer_rsrc((void*)(w == 3 ? (const char*)PTR(kb, 4) - 52224 : ixb), (short)0, w == 3 ? 53248 : 51200, SRD3);
  unsigned tc[2];
  tc[0] = __builtin_amdgcn_raw_buffer_load_b32(rsT, t0v, 0, 0);
  tc[1] = __builtin_amdgcn_raw_buffer_load_b32(rsI, t1v, 0, 0);
  const unsigned f0 = __builtin_amdgcn_raw_buffer_load_b32(rsF, mv, 0, SC1);
  SB();
  u32x4 P[4];
#define WSTEP(n) _Pragma("unroll") for (int j = 0; j < 4; ++j) { if (n == 0) P[j] = wld(j, rsW, wv, w0off); else c.R[n & 3][j] = wld(j, rsW, wv, f * 16 * FROW + (((n - 1) & 3) ^ rot) * 8192); } SB();
  if (NPRE > 0) { WSTEP(0) }
  if (NPRE > 1) { WSTEP(1) }
  if (NPRE > 2) { WSTEP(2) }
  if (NPRE > 3) { WSTEP(3) }
  if (NPRE > 4) { WSTEP(4) }
  TS(1);
  {
    LAS char* d = (LAS char*)smem + w * 16 * TPIECE;
#pragma unroll
    for (int k = 0; k < 16; ++k)
      __builtin_amdgcn_raw_ptr_buffer_load_lds(rsT, (LAS void*)(d + k * TPIECE), 16, tv, k * 4096, 0, 0);
  }
  SB();
  const unsigned T = (unsigned)__builtin_amdgcn_ballot_w64(((f0 >> (lane & 31)) & 1) == 0);
  __builtin_amdgcn_raw_buffer_store_b32(T, rsF, nv, 0, SC1);
  SB();
  TS(2);
  {
    const int iv = w * 13312 + lane * 16;
    LAS char* e = (LAS char*)smem + IDX_LDS + w * 13312;
#pragma unroll
    for (int k = 0; k < 4; ++k) {
      __builtin_amdgcn_raw_ptr_buffer_load_lds(k < 3 ? rsI : rsK, (LAS void*)(e + k * 4096), 16, iv + k * 4096, 0, 0, 0);
      if (k < 3) {
        __builtin_amdgcn_raw_ptr_buffer_load_lds(rsI, (LAS void*)(e + k * 4096), 16, iv + k * 4096, 0, 1024, 0);
        __builtin_amdgcn_raw_ptr_buffer_load_lds(rsI, (LAS void*)(e + k * 4096), 16, iv + k * 4096, 0, 2048, 0);
        __builtin_amdgcn_raw_ptr_buffer_load_lds(k < 2 ? rsI : rsJ, (LAS void*)(e + k * 4096), 16, iv + k * 4096, 0, 3072, 0);
      }
    }
  }
  SB();
  if (NPRE < 2 && NMID >= 2) { WSTEP(1) }
  if (NPRE < 3 && NMID >= 3) { WSTEP(2) }
  if (NPRE < 4 && NMID >= 4) { WSTEP(3) }
  if (NPRE < 5 && NMID >= 5) { WSTEP(4) }
  c.selA = (w & 2) ? 0x07060302u : 0x05040100u;
  c.selB = (w & 2) ? 0x05040100u : 0x07060302u;
  const unsigned ib = IDX_LDS + r * 400 + g * 4;
  const unsigned ib0 = ib + (w & 1) * 6400, ib1 = ib - (w & 1) * 6400;
#pragma unroll
  for (int u = 0; u < 8; ++u) { pk(c, c.F[0], P, u); SB(); asm volatile("s_nop 0"); SB(); xc(c, c.F[0], u); SB(); }
  asm volatile("" :: "v"(tc[0]), "v"(tc[1]));
#define CLEARS() { const u32x4 ones = {~0u, ~0u, ~0u, ~0u}; _Pragma("unroll") for (int b = 0; b < 4; ++b) __builtin_amdgcn_raw_buffer_store_b128(ones, rsS, cv[b], 0, SC1); SB(); }
  int cv[4];
#pragma unroll
  for (int b = 0; b < 4; ++b) cv[b] = me * 4096 + w * 1024 + lane * 16 + b * (1 << 20);
  asm volatile("" : "+v"(cv[0]), "+v"(cv[1]), "+v"(cv[2]), "+v"(cv[3]));
  if (CLR == 0) CLEARS()
  SB();
  const int w2_ = (w & 2) | (me >> 4), pj = 16 * (w & 1) + lane;
  Lp l;
  l.hv = lane < 16 ? lane * 4 : OOB;
  l.cn = 8192 + w2_ * 128 + 64 * (w & 1);
  l.cm = me * 256 + w * 64;
  l.ev = lane < 16 ? pj * 256 + w2_ * 64 + (me & 15) * 4 : OOB;
  l.hs = 0; l.nn = 0; l.ee = 0; l.n = 0; l.so = 0;
#pragma unroll
  for (int u = 0; u < 4; ++u) l.sw[u] = (u ^ rot) * 8192;
  l.f = f == 5 ? 0 : f + 1;
  l.wb = wv; l.wv = wv + l.f * (16 * FROW);
  l.ib0 = ib0 + w * 16; l.ib1 = ib1 + w * 16;
  l.ia0 = l.ib0 + l.f * 64; l.ia1 = l.ib1 + l.f * 64;
  unsigned ic0 = ib0 + f * 64 + w * 16, ic1 = ib1 + f * 64 + w * 16;
  asm volatile("" : "+v"(l.hv), "+v"(l.ev), "+v"(l.wv), "+v"(l.ib0), "+v"(l.ib1), "+v"(l.ia0), "+v"(l.ia1), "+v"(ic0), "+v"(ic1));
  TS(3);
  SB();
  asm volatile("s_waitcnt vmcnt(%0)" :: "n"((CLR == 0 ? 4 : 0) + 4 * (NMID - NPRE)) : "memory");
  __builtin_amdgcn_s_barrier();
  FENCE();
  TS(4);
#pragma unroll
  for (int mb = 0; mb < 8; ++mb) c.ad[mb] = lds32(((mb & 1) ? ib1 : ib0) + 384 + mb * 6400);
#pragma unroll
  for (int mb = 0; mb < 8; ++mb) c.I[mb] = lds32(((mb & 1) ? ic1 : ic0) + mb * 6400);
  SB();
  if (NMID < 3) { WSTEP(2) }
#pragma unroll
  for (int mb = 0; mb < 8; ++mb) c.T[mb] = lds128(taddr(c.ad[mb]) + w * 16);
  SB();
  if (NMID < 4) { WSTEP(3) }
  if (NMID < 5) { WSTEP(4) }
  if (CLR == 1) CLEARS()
  TS(5);

  do {
    step<0>(c, l, rsW, rsF);
    step<1>(c, l, rsW, rsF);
    step<2>(c, l, rsW, rsF);
    step<3>(c, l, rsW, rsF);
  } while (l.n < 5);
  unsigned n3 = l.hs, nn = l.nn;
  const int ev = l.ev;
  TS(6);
  if (__builtin_expect((__builtin_amdgcn_ballot_w64(l.ee != T) & 0xFFFFu) != 0, 0)) {
#pragma unroll 1
    for (int it = 0; it < (1 << 16); ++it) {
      const unsigned e = __builtin_amdgcn_raw_buffer_load_b32(rsF, l.hv, l.cm, SC1);
      asm volatile("s_waitcnt vmcnt(0)" ::: "memory");
      n3 = __builtin_amdgcn_raw_buffer_load_b32(rsF, l.hv, l.cn, SC1);
      __builtin_amdgcn_raw_buffer_store_b32(n3, rsF, n3 != nn ? ev : OOB, 0, SC1);
      nn = n3;
      if ((__builtin_amdgcn_ballot_w64(e != T) & 0xFFFFu) == 0) break;
    }
  }
  unsigned short xv = 0, wv_ = 0, bwv = 0, b2v = 0;
  SB();
#pragma unroll
  for (int k = 0; k < 32; ++k) {
    const int mb = k >> 2, nb = k & 3;
    c.acc[mb][nb] = mfma(c.F[0][nb], c.T[mb], c.acc[mb][nb]);
    SB();
    {
      const int u0 = (2 * k) / 3, ph = (2 * k) % 3;
      if (ph != 0 && u0 < 21) xc(c, t.G[u0 >> 3], u0 & 7);
      const int u1 = ph == 2 ? u0 + 1 : u0;
      if (ph != 1 && u1 < 21) pk(c, t.G[u1 >> 3], c.R[1 + (u1 >> 3)], u1 & 7);
    }
    if (k == 8 || k == 9) c.ad[k - 8] = taddr(c.I[k - 8]);
    if (k >= 10 && k < 18) t.TT[0][k & 1][(k - 10) >> 1] = lds128(c.ad[k & 1] + 16 * ((k - 10) >> 1));
    if (k == 20) xv = *(const GAS unsigned short*)(xw + (unsigned)kr);
    if (k == 23) wv_ = *(const GAS unsigned short*)(ww + (unsigned)kx);
    if (k == 26) bwv = *(const GAS unsigned short*)(bw + (unsigned)zv);
    if (k == 29) b2v = *(const GAS unsigned short*)(b2 + (unsigned)zv);
    SB();
  }
  TS(7);
  quarter<0>(c, t, rsS);
  quarter<1>(c, t, rsS);
  quarter<2>(c, t, rsS);
  quarter<3>(c, t, rsS);
  TS(8);
#pragma unroll
  for (int k = 0; k < 6; ++k) xsum<3>(c, t, k & 1, k < 2 ? 1 : (k < 4 ? 0 : 2));
  xpub<3>(t, rsS);
  SB();
  TS(9);
  __builtin_amdgcn_raw_buffer_store_b32(n3, rsF, n3 != nn ? ev : OOB, 0, SC1);

  const int iv = pollv;
  u32x4 v[4];
#pragma unroll
  for (int k = 0; k < 4; ++k) v[k] = __builtin_amdgcn_raw_buffer_load_b128(rsS, iv + k * 512, 0, SC1);
  SB();
  u32x2 b1v[4], w2v[4];
#pragma unroll
  for (int e = 0; e < 4; ++e) { b1v[e] = *(const LAS u32x2*)(size_t)(cb + 16 * e); w2v[e] = *(const LAS u32x2*)(size_t)(cb + 1024 + 16 * e); }
  const unsigned sh = (lane >> 5) * 16;
  float bb[8], w2[8];
#pragma unroll
  for (int k = 0; k < 8; ++k) {
    bb[k] = __uint_as_float((b1v[k >> 1][k & 1] >> sh) << 16);
    w2[k] = __uint_as_float((w2v[k >> 1][k & 1] >> sh) << 16);
  }
  const float wide = rbf(rbf(row_sum16(r < NW ? bf2f(xv) * bf2f(wv_) : 0.f)) + bf2f(bwv));
  const float b2f_ = bf2f(b2v);
  SB();
#pragma unroll 1
  for (int it = 0; it < (1 << 16); ++it) {
    unsigned m = 0;
#pragma unroll
    for (int k = 0; k < 4; ++k) m = max(max(m, max(v[k][0], v[k][1])), max(v[k][2], v[k][3]));
    if (__builtin_amdgcn_ballot_w64(m == 0xFFFFFFFFu) == 0) break;
    FENCE();
#pragma unroll
    for (int k = 0; k < 4; ++k) v[k] = __builtin_amdgcn_raw_buffer_load_b128(rsS, iv + k * 512, 0, SC1);
  }
  TS(10);
  float a = 0.f;
#pragma unroll
  for (int k = 0; k < 8; ++k) {
    const int e = k >> 1;
    float p[4];
#pragma unroll
    for (int j = 0; j < 4; ++j) p[j] = __uint_as_float((k & 1) ? (v[j][e] & 0xFFFF0000u) : (v[j][e] << 16));
    float hh = rbf(rbf((p[0] + p[1]) + (p[2] + p[3])) + bb[k]);
    hh = hh > 0.f ? hh : hh * 0.f;
    a += hh * w2[k];
  }
  a = row_sum16(a);
  auto p16 = __builtin_amdgcn_permlane16_swap(__float_as_uint(a), __float_as_uint(a), false, false);
  a = __uint_as_float(p16[0]) + __uint_as_float(p16[1]);
  auto p32 = __builtin_amdgcn_permlane32_swap(__float_as_uint(a), __float_as_uint(a), false, false);
  a = __uint_as_float(p32[0]) + __uint_as_float(p32[1]);
  const float z = rbf(wide + rbf(rbf(a) + b2f_));
  if (lane == 0) out[row] = (__bf16)(1.f / (1.f + __expf(-z)));
  TS(11);
}

extern "C" void kernel_launch(void* const* d_in, const int* in_sizes, int n_in,
                              void* d_out, int out_size, void* d_ws, size_t ws_size,
                              hipStream_t stream) {
  const int* idx = (const int*)d_in[0];
  const unsigned short* table = (const unsigned short*)d_in[1];
  const unsigned short* xw = (const unsigned short*)d_in[2];
  const unsigned short* ww = (const unsigned short*)d_in[3];
  const unsigned short* bw = (const unsigned short*)d_in[4];
  const unsigned short* W1 = (const unsigned short*)d_in[5];
  const unsigned short* b1 = (const unsigned short*)d_in[6];
  const unsigned short* W2 = (const unsigned short*)d_in[7];
  const unsigned short* b2 = (const unsigned short*)d_in[8];
  (void)hipFuncSetAttribute((const void*)fused_kernel, hipFuncAttributeMaxDynamicSharedMemorySize, LDS_BYTES);
  fused_kernel<<<256, 256, LDS_BYTES, stream>>>(idx, table, W1, (char*)d_ws, xw, ww, bw, b1, W2, b2, d_out, d_ws);
}
```

```cpp
#include <hip/hip_runtime.h>
#include <hip/hip_bf16.h>

typedef short    bf16x8 __attribute__((ext_vector_type(8)));
typedef __bf16   bf16x2 __attribute__((ext_vector_type(2)));
typedef float    f32x4  __attribute__((ext_vector_type(4)));
typedef float    f32x2  __attribute__((ext_vector_type(2)));
typedef unsigned u32x4  __attribute__((ext_vector_type(4)));
typedef unsigned u32x8  __attribute__((ext_vector_type(8)));
typedef unsigned u32x16 __attribute__((ext_vector_type(16)));
#define GAS __attribute__((address_space(1)))
typedef unsigned u32x2  __attribute__((ext_vector_type(2)));
#define LAS __attribute__((address_space(3)))
#define SB() __builtin_amdgcn_sched_barrier(0)
#define FENCE() asm volatile("" ::: "memory")

constexpr int NPRE = 3;
constexpr int NMID = 4;
constexpr int ROT = 0;
constexpr int CLR = 1;
constexpr int NF = 100, ND = 128, NV = 1000, NW = 13, NH = 512;
constexpr int TPIECE = 1040;
constexpr int IDX_LDS = 64 * TPIECE;
constexpr int X1_LDS = IDX_LDS;
constexpr int X0_OFF = 52 * 1024;
constexpr int LDS_BYTES = X1_LDS + X0_OFF + 24576;
constexpr int FROW = ND * NH * 2;
constexpr int SC1 = 16;
constexpr int SRD3 = 0x00020000;
constexpr int OOB = 0x40000000;

__device__ __forceinline__ float rbf(float x) { return (float)(__bf16)x; }
__device__ __forceinline__ float bf2f(unsigned short h) { return __uint_as_float((unsigned)h << 16); }
template <int C> __device__ __forceinline__ float dpp_add(float x) {
  return x + __uint_as_float(__builtin_amdgcn_update_dpp(0, __float_as_uint(x), C, 0xf, 0xf, true));
}
__device__ __forceinline__ float row_sum16(float x) {
  x = dpp_add<0xB1>(x);
  x = dpp_add<0x4E>(x);
  x = dpp_add<0x141>(x);
  x = dpp_add<0x140>(x);
  return x;
}
__device__ __forceinline__ u32x4 lds128(unsigned a) { return *(const LAS u32x4*)(size_t)a; }
__device__ __forceinline__ f32x4 ldsf4(unsigned a) { return *(const LAS f32x4*)(size_t)a; }
__device__ __forceinline__ unsigned lds32(unsigned a) { return *(const LAS unsigned*)(size_t)a; }
__device__ __forceinline__ unsigned taddr(unsigned v) { return (v << 6) + (v & ~15u); }
__device__ __forceinline__ f32x4 mfma(u32x4 a, u32x4 b, f32x4 c) {
  return __builtin_amdgcn_mfma_f32_16x16x32_bf16(__builtin_bit_cast(bf16x8, a), __builtin_bit_cast(bf16x8, b), c, 0, 0, 0);
}

__device__ __forceinline__ void mfa(f32x4& c, const u32x4& a, const u32x4& b) {
  asm volatile("v_mfma_f32_16x16x32_bf16 %0, %1, %2, %0" : "+a"(c) : "v"(a), "v"(b));
  SB();
}

struct St {
  f32x4 acc[8][4];
  u32x4 T[8];
  u32x4 F[2][4];
  u32x4 R[4][4];
  unsigned ad[8], I[8];
  unsigned selA, selB, pa;
};
__device__ __forceinline__ void pk(St& c, u32x4* F, const u32x4* R, int u) {
  const int p = u >> 2, nb = u & 3;
  F[nb][2 + p] = __builtin_amdgcn_perm(R[2 * p + 1][2 + (nb >> 1)], R[2 * p][2 + (nb >> 1)], (nb & 1) ? c.selB : c.selA);
  SB();
  c.pa = __builtin_amdgcn_perm(R[2 * p + 1][nb >> 1], R[2 * p][nb >> 1], (nb & 1) ? c.selB : c.selA);
}
__device__ __forceinline__ void xc(St& c, u32x4* F, int u) {
  const int p = u >> 2, nb = u & 3;
  unsigned lo = 0, hi = F[nb][2 + p];
#if defined(__HIP_DEVICE_COMPILE__)
  register unsigned long long lm asm("vcc") = 0x00FF00FF00FF00FFull;
  asm volatile("v_cndmask_b32_dpp %0, %1, %2, vcc row_ror:8 row_mask:0xf bank_mask:0xf"
               : "=v"(lo) : "v"(hi), "v"(c.pa), "s"(lm));
#endif
  SB();
  asm volatile("v_mov_b32_dpp %0, %1 row_ror:8 row_mask:0xf bank_mask:0x3" : "+v"(hi) : "v"(c.pa));
  F[nb][p] = lo; F[nb][2 + p] = hi;
}
__device__ __forceinline__ u32x4 wld(int j, __amdgpu_buffer_rsrc_t rs, int voff, int soff) {
  return __builtin_amdgcn_raw_buffer_load_b128(rs, voff + j * 1024, soff, 0);
}

struct Lp {
  int n, f, wb, wv, hv, ev, cn, cm, so, sw[4];
  unsigned ib0, ib1, ia0, ia1, hs, nn, ee;
};
template <int U>
__device__ __forceinline__ void step(St& c, Lp& l, __amdgpu_buffer_rsrc_t rsW, __amdgpu_buffer_rsrc_t rsF) {
  constexpr int CUR = U & 1, NX = CUR ^ 1, SL = (U + 1) & 3, SN = (U + 2) & 3;
#pragma unroll
  for (int i = 0; i < 4; ++i) {
    const int m0 = 2 * i, m1 = 2 * i + 1;
    mfa(c.acc[m0][0], c.F[CUR][0], c.T[m0]);
    pk(c, c.F[NX], c.R[SL], m0);
    SB();
    mfa(c.acc[m0][1], c.F[CUR][1], c.T[m0]);
    xc(c, c.F[NX], m0);
    SB();
    mfa(c.acc[m0][2], c.F[CUR][2], c.T[m0]);
    pk(c, c.F[NX], c.R[SL], m1);
    SB();
    mfa(c.acc[m0][3], c.F[CUR][3], c.T[m0]);
    xc(c, c.F[NX], m1);
    SB();
    mfa(c.acc[m1][0], c.F[CUR][0], c.T[m1]);
    if (U == 0) c.ad[m0] = taddr(c.I[m0]);
    if (U == 1) {
      const int m = (i & 1) + 4 * (i >> 1);
      c.I[m] = lds32(((i & 1) ? l.ia1 : l.ia0) + m * 6400);
      c.I[m + 2] = lds32(((i & 1) ? l.ia1 : l.ia0) + (m + 2) * 6400);
    }
    if (U == 2 && i == 0) l.so = l.n == 2 ? 0 : OOB;
    if (U == 2 && i == 1) l.so = ((18 >> l.n) & 1) ? l.cn : OOB;
    if (U == 2 && i == 2) l.so = l.n == 3 ? l.cm : l.so;
    if (U == 3 && i == 0) l.f = l.f + 1;
    if (U == 3 && i == 1) l.f = l.f == 6 ? 0 : l.f;
    if (U == 3 && i == 2) { l.ia0 = l.ib0 + l.f * 64; l.ia1 = l.ib1 + l.f * 64; asm volatile("" : "+v"(l.ia0), "+v"(l.ia1)); }
    if (U == 3 && i == 3) { l.n = l.n + 1; asm volatile("" : "+s"(l.n)); }
    if (U == 2 || U == 3) asm volatile("" : "+s"(l.so), "+s"(l.f));
    SB();
    mfa(c.acc[m1][1], c.F[CUR][1], c.T[m1]);
    c.T[m0] = lds128(c.ad[m0] + 16 * U);
    if (i & 1) c.R[SL][i - 1] = wld(i - 1, rsW, l.wv, l.sw[U]);
    if (U == 2 && i == 0) __builtin_amdgcn_raw_buffer_store_b32(l.hs, rsF, l.ev, l.so, SC1);
    if (U == 2 && i == 2) l.hs = __builtin_amdgcn_raw_buffer_load_b32(rsF, l.hv, l.so, SC1);
    SB();
    mfa(c.acc[m1][2], c.F[CUR][2], c.T[m1]);
    if (U == 0) c.ad[m1] = taddr(c.I[m1]);
    if (U == 2 && i == 0) { l.nn |= l.ee; l.ee = l.hs; asm volatile("" : "+v"(l.nn), "+v"(l.ee)); }
    if (i & 1) c.R[SL][i] = wld(i, rsW, l.wv, l.sw[U]);
    if (U == 3 && i == 3) { SB(); l.wv = l.wb + l.f * (16 * FROW); asm volatile("" : "+v"(l.wv)); }
    SB();
    mfa(c.acc[m1][3], c.F[CUR][3], c.T[m1]);
    c.T[m1] = lds128(c.ad[m1] + 16 * U);
    SB();
    if (i == 1)     asm volatile("" :: "v"(c.T[m0 + 2]), "v"(c.T[m1 + 2]), "v"(c.R[SL][2]), "v"(c.R[SL][3]));
    else if (i < 3) asm volatile("" :: "v"(c.T[m0 + 2]), "v"(c.T[m1 + 2]));
    else            asm volatile("" :: "v"(c.T[0]), "v"(c.T[1]), "v"(c.R[SN][0]), "v"(c.R[SN][1]));
    SB();
  }
}

struct Tl {
  u32x4 G[4][4];
  u32x4 TT[2][2][4];
  f32x4 rd[3][2];
  f32x4 s[2];
  unsigned xa[3], xr;
  int pv;
};
template <int Q> __device__ __forceinline__ void xwrite(St& c, Tl& t, int k, int h) {
  *(LAS f32x4*)(size_t)(t.xa[k - 1] + ((Q & 1) ? 0 : X0_OFF) + h * 1024) = c.acc[2 * Q + (k & 1)][2 * h + (k >> 1)];
}
template <int Q> __device__ __forceinline__ void xread(Tl& t, int k, int h) {
  t.rd[k][h] = ldsf4(t.xr + ((Q & 1) ? 0 : X0_OFF) + k * 2048 + h * 1024);
}
template <int Q> __device__ __forceinline__ void xsum(St& c, Tl& t, int h, int ph) {
  if (ph == 0) { asm volatile("" : "+a"(c.acc[2 * Q][2 * h])); t.s[h] = c.acc[2 * Q][2 * h] + t.rd[0][h]; }
  if (ph == 1) t.rd[1][h] += t.rd[2][h];
  if (ph == 2) t.s[h] += t.rd[1][h];
}
template <int Q> __device__ __forceinline__ void xpub(Tl& t, __amdgpu_buffer_rsrc_t rsS) {
  u32x4 o;
#pragma unroll
  for (int e = 0; e < 4; ++e) {
    const f32x2 p = {t.s[0][e], t.s[1][e]};
    o[e] = min(__builtin_bit_cast(unsigned, __builtin_convertvector(p, bf16x2)), 0xFFFFFFFEu);
  }
  __builtin_amdgcn_raw_buffer_store_b128(o, rsS, t.pv + Q * (1 << 20), 0, SC1);
}
__device__ __forceinline__ void wgbar() {
  asm volatile("s_waitcnt lgkmcnt(0)" ::: "memory");
  __builtin_amdgcn_s_barrier();
  FENCE();
}

template <int Q>
__device__ __forceinline__ void quarter(St& c, Tl& t, __amdgpu_buffer_rsrc_t rsS) {
  constexpr int B = Q & 1;
#pragma unroll
  for (int k = 0; k < 32; ++k) {
    int ch, u, nb;
    if (Q < 3) { ch = k >> 3; u = (k >> 2) & 1; nb = k & 3; }
    else { const int pr = k >> 3; ch = (k >> 1) & 3; u = pr < 2 ? 1 : 0; nb = 2 * (k & 1) + ((pr == 1 || pr == 2) ? 1 : 0); }
    c.acc[2 * Q + u][nb] = mfma(t.G[ch][nb], t.TT[B][u][ch], c.acc[2 * Q + u][nb]);
    SB();
    if constexpr (Q == 0) {
      if (k < 22) {
        if (k & 1) xc(c, t.G[k < 6 ? 2 : 3], k < 6 ? 5 + (k >> 1) : (k - 6) >> 1);
        else       pk(c, t.G[k < 6 ? 2 : 3], c.R[k < 6 ? 3 : 0], k < 6 ? 5 + (k >> 1) : (k - 6) >> 1);
      }
      if (k == 8 || k == 9) c.ad[k - 6] = taddr(c.I[k - 6]);
      if (k >= 10 && k < 14) t.TT[1][k & 1][(k - 10) >> 1] = lds128(c.ad[2 + (k & 1)] + 16 * ((k - 10) >> 1));
      if (k >= 22 && k < 26) t.TT[1][k & 1][(k - 18) >> 1] = lds128(c.ad[2 + (k & 1)] + 16 * ((k - 18) >> 1));
      if (k >= 26 && k < 30) c.ad[k - 22] = taddr(c.I[k - 22]);
    } else {
      if (k >= 1 && k < 7) xwrite<Q - 1>(c, t, 1 + (k - 1) / 2, (k - 1) & 1);
      if (k == 9) wgbar();
      if (k >= 10 && k < 13) { xread<Q - 1>(t, k - 10, 0); xread<Q - 1>(t, k - 10, 1); }
      if (Q < 3) {
        if (k >= 13 && k < 21) t.TT[B ^ 1][(k - 13) & 1][(k - 13) >> 1] = lds128(c.ad[2 * Q + 2 + ((k - 13) & 1)] + 16 * ((k - 13) >> 1));
        if (k >= 21 && k < 27) xsum<Q - 1>(c, t, (k - 21) & 1, (k - 21) >> 1);
        if (k == 27) xpub<Q - 1>(t, rsS);
      } else {
        if (k == 13 || k == 14) xwrite<3>(c, t, 1, k - 13);
        if (k == 17 || k == 18) xwrite<3>(c, t, 3, k - 17);
        if (k >= 19 && k < 25) xsum<Q - 1>(c, t, (k - 19) & 1, (k - 19) >> 1);
        if (k == 25 || k == 26) xwrite<3>(c, t, 2, k - 25);
        if (k == 27) xpub<Q - 1>(t, rsS);
        if (k == 30) wgbar();
        if (k == 30) { xread<3>(t, 1, 0); xread<3>(t, 2, 0); xread<3>(t, 1, 1); xread<3>(t, 2, 1); }
        if (k == 31) { xread<3>(t, 0, 0); xread<3>(t, 0, 1); }
      }
    }
    SB();
  }
}

__global__ void __launch_bounds__(256, 1)
fused_kernel(const void* a0, const void* a1, const void* a2, void* a3, const void* a4, const void* a5, const void* a6,
             const void* a7, const void* a8, const void* a9, void* a10, void* a11) {
  extern __shared__ __attribute__((aligned(16))) char smem[];
  const int tid = threadIdx.x, lane = tid & 63;
  const int w = __builtin_amdgcn_readfirstlane(tid >> 6);
  const int bid = blockIdx.x;
  u32x8 ka; u32x16 kb;
  asm volatile("s_load_dwordx8 %0, %2, 0x0\n\ts_load_dwordx16 %1, %2, 0x20" : "=&s"(ka), "=&s"(kb) : "s"(__builtin_amdgcn_kernarg_segment_ptr()));
  SB();
  const int xcd = bid & 7, jj = bid >> 3;
  const int q = xcd >> 1, nt = 4 * (xcd & 1) + (jj & 3), mt = jj >> 2;
  const int me = q * 8 + nt;
  const int r = lane & 15, g = lane >> 4;

  St c;
  {
    u32x4 z = {0u, 0u, 0u, 0u};
    asm volatile("s_nop 1" : "+v"(z));
#pragma unroll
    for (int k = 0; k < 32; ++k) asm volatile("v_mfma_f32_16x16x32_bf16 %0, %1, %1, 0" : "=a"(c.acc[k >> 2][k & 3]) : "v"(z));
  }
  const int wbase = (4 * w * ND + 32 * q) * 1024 + nt * 128;
  const int f = mt >= 6 ? mt - 6 : mt;
  const int rot = ROT;
  const int w0off = (96 - 4 * w) * FROW + (w ^ rot) * 8192;
  int t0v = (jj * 32 + w * 8 + lane) * 256 + q * 64, t1v = ((jj & 3) * 100 + w * 25 + lane) * 128;
  asm volatile("" : "+v"(t0v), "+v"(t1v));
  t0v = lane < 8 ? t0v : OOB;
  t1v = lane < 25 ? t1v : OOB;
  int mv = me * 256 + w * 64 + lane * 4;
  const int nme = 8192 + w * 128 + me * 4;
  mv = lane < 16 ? mv : OOB;
  mv = lane == 16 ? nme : mv;
  int nv = lane == 0 ? nme : OOB;
  int tv = (256 * w + (lane >> 2)) * 256 + q * 64 + ((lane & 3) ^ rot) * 16;
  int wv = g * FROW + (r >> 3) * 4096 + (r & 7) * 16;
  asm volatile("" : "+v"(t0v), "+v"(t1v), "+v"(wv), "+v"(mv), "+v"(tv), "+v"(nv));
  Tl t;
  const int row = mt * 128 + 32 * w + me;
  int kx = r < NW ? 2 * r : 0, kr = kx + row * (2 * NW), zv = 0;
#pragma unroll
  for (int k = 0; k < 3; ++k) t.xa[k] = X1_LDS + ((w ^ (k + 1)) * 3 + k) * 2048 + lane * 16;
  t.xr = X1_LDS + w * 6144 + lane * 16;
  t.pv = (16 * (w & 1) + r) * 4096 + (w >> 1) * 2048 + me * 64 + g * 16;
  int pollv = w * (1 << 20) + me * 4096 + (lane >> 5) * 2048 + (lane & 31) * 16;
  unsigned cb = IDX_LDS + 51200 + (lane & 28) * 32 + (lane & 1) * 64 + (lane & 2) * 4;
  asm volatile("" : "+v"(kx), "+v"(kr), "+v"(zv), "+v"(t.xa[0]), "+v"(t.xa[1]), "+v"(t.xa[2]), "+v"(t.xr), "+v"(t.pv), "+v"(pollv), "+v"(cb));
  SB();
  asm volatile("s_waitcnt lgkmcnt(0)" : "+s"(ka), "+s"(kb));
#define PTR(v, i) (((unsigned long long)v[2 * (i) + 1] << 32) | v[2 * (i)])
  const char* idx = (const char*)PTR(ka, 0); const char* W1 = (const char*)PTR(ka, 2); char* slab = (char*)PTR(ka, 3);
  const GAS char* xw = (const GAS char*)PTR(kb, 0);
  const GAS char* ww = (const GAS char*)PTR(kb, 1);
  const GAS char* bw = (const GAS char*)PTR(kb, 2);
  const GAS char* b2 = (const GAS char*)PTR(kb, 5);
  GAS __bf16* out = (GAS __bf16*)PTR(kb, 6);
  const __amdgpu_buffer_rsrc_t rsW = __builtin_amdgcn_make_buffer_rsrc((void*)(W1 + wbase), (short)0, NF * FROW - wbase, SRD3);
  const __amdgpu_buffer_rsrc_t rsT = __builtin_amdgcn_make_buffer_rsrc((void*)PTR(ka, 1), (short)0, NV * ND * 2, SRD3);
  const __amdgpu_buffer_rsrc_t rsI = __builtin_amdgcn_make_buffer_rsrc((void*)(idx + mt * 128 * NF * 4), (short)0, 128 * NF * 4, SRD3);
  const __amdgpu_buffer_rsrc_t rsS = __builtin_amdgcn_make_buffer_rsrc((void*)(slab + mt * 131072), (short)0, (4 << 20) - mt * 131072, SRD3);
  const __amdgpu_buffer_rsrc_t rsF = __builtin_amdgcn_make_buffer_rsrc((void*)(slab + (4 << 20) + mt * 16384), (short)0, 8704, SRD3);
  const char* ixb = idx + mt * 128 * NF * 4;
  const __amdgpu_buffer_rsrc_t rsJ = __builtin_amdgcn_make_buffer_rsrc((void*)(w == 3 ? (const char*)PTR(kb, 3) - 51200 : ixb), (short)0, w == 3 ? 52224 : 51200, SRD3);
  const __amdgpu_buffer_rsrc_t rsK = __builtin_amdgcn_make_buffer_rsrc((void*)(w == 3 ? (const char*)PTR(kb, 4) - 52224 : ixb), (short)0, w == 3 ? 53248 : 51200, SRD3);
  unsigned tc[2];
  tc[0] = __builtin_amdgcn_raw_buffer_load_b32(rsT, t0v, 0, 0);
  tc[1] = __builtin_amdgcn_raw_buffer_load_b32(rsI, t1v, 0, 0);
  const unsigned f0 = __builtin_amdgcn_raw_buffer_load_b32(rsF, mv, 0, SC1);
  SB();
  u32x4 P[4];
#define WSTEP(n) _Pragma("unroll") for (int j = 0; j < 4; ++j) { if (n == 0) P[j] = wld(j, rsW, wv, w0off); else c.R[n & 3][j] = wld(j, rsW, wv, f * 16 * FROW + (((n - 1) & 3) ^ rot) * 8192); } SB();
  if (NPRE > 0) { WSTEP(0) }
  if (NPRE > 1) { WSTEP(1) }
  if (NPRE > 2) { WSTEP(2) }
  if (NPRE > 3) { WSTEP(3) }
  if (NPRE > 4) { WSTEP(4) }
  {
    LAS char* d = (LAS char*)smem + w * 16 * TPIECE;
#pragma unroll
    for (int k = 0; k < 16; ++k)
      __builtin_amdgcn_raw_ptr_buffer_load_lds(rsT, (LAS void*)(d + k * TPIECE), 16, tv, k * 4096, 0, 0);
  }
  SB();
  const unsigned T = (unsigned)__builtin_amdgcn_ballot_w64(((f0 >> (lane & 31)) & 1) == 0);
  __builtin_amdgcn_raw_buffer_store_b32(T, rsF, nv, 0, SC1);
  SB();
  {
    const int iv = w * 13312 + lane * 16;
    LAS char* e = (LAS char*)smem + IDX_LDS + w * 13312;
#pragma unroll
    for (int k = 0; k < 4; ++k) {
      __builtin_amdgcn_raw_ptr_buffer_load_lds(k < 3 ? rsI : rsK, (LAS void*)(e + k * 4096), 16, iv + k * 4096, 0, 0, 0);
      if (k < 3) {
        __builtin_amdgcn_raw_ptr_buffer_load_lds(rsI, (LAS void*)(e + k * 4096), 16, iv + k * 4096, 0, 1024, 0);
        __builtin_amdgcn_raw_ptr_buffer_load_lds(rsI, (LAS void*)(e + k * 4096), 16, iv + k * 4096, 0, 2048, 0);
        __builtin_amdgcn_raw_ptr_buffer_load_lds(k < 2 ? rsI : rsJ, (LAS void*)(e + k * 4096), 16, iv + k * 4096, 0, 3072, 0);
      }
    }
  }
  SB();
  if (NPRE < 2 && NMID >= 2) { WSTEP(1) }
  if (NPRE < 3 && NMID >= 3) { WSTEP(2) }
  if (NPRE < 4 && NMID >= 4) { WSTEP(3) }
  if (NPRE < 5 && NMID >= 5) { WSTEP(4) }
  c.selA = (w & 2) ? 0x07060302u : 0x05040100u;
  c.selB = (w & 2) ? 0x05040100u : 0x07060302u;
  const unsigned ib = IDX_LDS + r * 400 + g * 4;
  const unsigned ib0 = ib + (w & 1) * 6400, ib1 = ib - (w & 1) * 6400;
#pragma unroll
  for (int u = 0; u < 8; ++u) { pk(c, c.F[0], P, u); SB(); asm volatile("s_nop 0"); SB(); xc(c, c.F[0], u); SB(); }
  asm volatile("" :: "v"(tc[0]), "v"(tc[1]));
#define CLEARS() { const u32x4 ones = {~0u, ~0u, ~0u, ~0u}; _Pragma("unroll") for (int b = 0; b < 4; ++b) __builtin_amdgcn_raw_buffer_store_b128(ones, rsS, cv[b], 0, SC1); SB(); }
  int cv[4];
#pragma unroll
  for (int b = 0; b < 4; ++b) cv[b] = me * 4096 + w * 1024 + lane * 16 + b * (1 << 20);
  asm volatile("" : "+v"(cv[0]), "+v"(cv[1]), "+v"(cv[2]), "+v"(cv[3]));
  if (CLR == 0) CLEARS()
  SB();
  const int w2_ = (w & 2) | (me >> 4), pj = 16 * (w & 1) + lane;
  Lp l;
  l.hv = lane < 16 ? lane * 4 : OOB;
  l.cn = 8192 + w2_ * 128 + 64 * (w & 1);
  l.cm = me * 256 + w * 64;
  l.ev = lane < 16 ? pj * 256 + w2_ * 64 + (me & 15) * 4 : OOB;
  l.hs = 0; l.nn = 0; l.ee = 0; l.n = 0; l.so = 0;
#pragma unroll
  for (int u = 0; u < 4; ++u) l.sw[u] = (u ^ rot) * 8192;
  l.f = f == 5 ? 0 : f + 1;
  l.wb = wv; l.wv = wv + l.f * (16 * FROW);
  l.ib0 = ib0 + w * 16; l.ib1 = ib1 + w * 16;
  l.ia0 = l.ib0 + l.f * 64; l.ia1 = l.ib1 + l.f * 64;
  unsigned ic0 = ib0 + f * 64 + w * 16, ic1 = ib1 + f * 64 + w * 16;
  asm volatile("" : "+v"(l.hv), "+v"(l.ev), "+v"(l.wv), "+v"(l.ib0), "+v"(l.ib1), "+v"(l.ia0), "+v"(l.ia1), "+v"(ic0), "+v"(ic1));
  SB();
  asm volatile("s_waitcnt vmcnt(%0)" :: "n"((CLR == 0 ? 4 : 0) + 4 * (NMID - NPRE)) : "memory");
  __builtin_amdgcn_s_barrier();
  FENCE();
#pragma unroll
  for (int mb = 0; mb < 8; ++mb) c.ad[mb] = lds32(((mb & 1) ? ib1 : ib0) + 384 + mb * 6400);
#pragma unroll
  for (int mb = 0; mb < 8; ++mb) c.I[mb] = lds32(((mb & 1) ? ic1 : ic0) + mb * 6400);
  SB();
  if (NMID < 3) { WSTEP(2) }
#pragma unroll
  for (int mb = 0; mb < 8; ++mb) c.T[mb] = lds128(taddr(c.ad[mb]) + w * 16);
  SB();
  if (NMID < 4) { WSTEP(3) }
  if (NMID < 5) { WSTEP(4) }
  if (CLR == 1) CLEARS()

  do {
    step<0>(c, l, rsW, rsF);
    step<1>(c, l, rsW, rsF);
    step<2>(c, l, rsW, rsF);
    step<3>(c, l, rsW, rsF);
  } while (l.n < 5);
  unsigned n3 = l.hs, nn = l.nn;
  const int ev = l.ev;
  if (__builtin_expect((__builtin_amdgcn_ballot_w64(l.ee != T) & 0xFFFFu) != 0, 0)) {
#pragma unroll 1
    for (int it = 0; it < (1 << 16); ++it) {
      const unsigned e = __builtin_amdgcn_raw_buffer_load_b32(rsF, l.hv, l.cm, SC1);
      asm volatile("s_waitcnt vmcnt(0)" ::: "memory");
      n3 = __builtin_amdgcn_raw_buffer_load_b32(rsF, l.hv, l.cn, SC1);
      __builtin_amdgcn_raw_buffer_store_b32(n3, rsF, n3 != nn ? ev : OOB, 0, SC1);
      nn = n3;
      if ((__builtin_amdgcn_ballot_w64(e != T) & 0xFFFFu) == 0) break;
    }
  }
  unsigned short xv = 0, wv_ = 0, bwv = 0, b2v = 0;
  SB();
#pragma unroll
  for (int k = 0; k < 32; ++k) {
    const int mb = k >> 2, nb = k & 3;
    c.acc[mb][nb] = mfma(c.F[0][nb], c.T[mb], c.acc[mb][nb]);
    SB();
    {
      const int u0 = (2 * k) / 3, ph = (2 * k) % 3;
      if (ph != 0 && u0 < 21) xc(c, t.G[u0 >> 3], u0 & 7);
      const int u1 = ph == 2 ? u0 + 1 : u0;
      if (ph != 1 && u1 < 21) pk(c, t.G[u1 >> 3], c.R[1 + (u1 >> 3)], u1 & 7);
    }
    if (k == 8 || k == 9) c.ad[k - 8] = taddr(c.I[k - 8]);
    if (k >= 10 && k < 18) t.TT[0][k & 1][(k - 10) >> 1] = lds128(c.ad[k & 1] + 16 * ((k - 10) >> 1));
    if (k == 20) xv = *(const GAS unsigned short*)(xw + (unsigned)kr);
    if (k == 23) wv_ = *(const GAS unsigned short*)(ww + (unsigned)kx);
    if (k == 26) bwv = *(const GAS unsigned short*)(bw + (unsigned)zv);
    if (k == 29) b2v = *(const GAS unsigned short*)(b2 + (unsigned)zv);
    SB();
  }
  quarter<0>(c, t, rsS);
  quarter<1>(c, t, rsS);
  quarter<2>(c, t, rsS);
  quarter<3>(c, t, rsS);
#pragma unroll
  for (int k = 0; k < 6; ++k) xsum<3>(c, t, k & 1, k < 2 ? 1 : (k < 4 ? 0 : 2));
  xpub<3>(t, rsS);
  SB();
  __builtin_amdgcn_raw_buffer_store_b32(n3, rsF, n3 != nn ? ev : OOB, 0, SC1);

  const int iv = pollv;
  u32x4 v[4];
#pragma unroll
  for (int k = 0; k < 4; ++k) v[k] = __builtin_amdgcn_raw_buffer_load_b128(rsS, iv + k * 512, 0, SC1);
  SB();
  u32x2 b1v[4], w2v[4];
#pragma unroll
  for (int e = 0; e < 4; ++e) { b1v[e] = *(const LAS u32x2*)(size_t)(cb + 16 * e); w2v[e] = *(const LAS u32x2*)(size_t)(cb + 1024 + 16 * e); }
  const unsigned sh = (lane >> 5) * 16;
  float bb[8], w2[8];
#pragma unroll
  for (int k = 0; k < 8; ++k) {
    bb[k] = __uint_as_float((b1v[k >> 1][k & 1] >> sh) << 16);
    w2[k] = __uint_as_float((w2v[k >> 1][k & 1] >> sh) << 16);
  }
  const float wide = rbf(rbf(row_sum16(r < NW ? bf2f(xv) * bf2f(wv_) : 0.f)) + bf2f(bwv));
  const float b2f_ = bf2f(b2v);
  SB();
#pragma unroll 1
  for (int it = 0; it < (1 << 16); ++it) {
    unsigned m = 0;
#pragma unroll
    for (int k = 0; k < 4; ++k) m = max(max(m, max(v[k][0], v[k][1])), max(v[k][2], v[k][3]));
    if (__builtin_amdgcn_ballot_w64(m == 0xFFFFFFFFu) == 0) break;
    FENCE();
#pragma unroll
    for (int k = 0; k < 4; ++k) v[k] = __builtin_amdgcn_raw_buffer_load_b128(rsS, iv + k * 512, 0, SC1);
  }
  float a = 0.f;
#pragma unroll
  for (int k = 0; k < 8; ++k) {
    const int e = k >> 1;
    float p[4];
#pragma unroll
    for (int j = 0; j < 4; ++j) p[j] = __uint_as_float((k & 1) ? (v[j][e] & 0xFFFF0000u) : (v[j][e] << 16));
    float hh = rbf(rbf((p[0] + p[1]) + (p[2] + p[3])) + bb[k]);
    hh = hh > 0.f ? hh : hh * 0.f;
    a += hh * w2[k];
  }
  a = row_sum16(a);
  auto p16 = __builtin_amdgcn_permlane16_swap(__float_as_uint(a), __float_as_uint(a), false, false);
  a = __uint_as_float(p16[0]) + __uint_as_float(p16[1]);
  auto p32 = __builtin_amdgcn_permlane32_swap(__float_as_uint(a), __float_as_uint(a), false, false);
  a = __uint_as_float(p32[0]) + __uint_as_float(p32[1]);
  const float z = rbf(wide + rbf(rbf(a) + b2f_));
  if (lane == 0) out[row] = (__bf16)(1.f / (1.f + __expf(-z)));
}

extern "C" void kernel_launch(void* const* d_in, const int* in_sizes, int n_in,
                              void* d_out, int out_size, void* d_ws, size_t ws_size,
                              hipStream_t stream) {
  const int* idx = (const int*)d_in[0];
  const unsigned short* table = (const unsigned short*)d_in[1];
  const unsigned short* xw = (const unsigned short*)d_in[2];
  const unsigned short* ww = (const unsigned short*)d_in[3];
  const unsigned short* bw = (const unsigned short*)d_in[4];
  const unsigned short* W1 = (const unsigned short*)d_in[5];
  const unsigned short* b1 = (const unsigned short*)d_in[6];
  const unsigned short* W2 = (const unsigned short*)d_in[7];
  const unsigned short* b2 = (const unsigned short*)d_in[8];
  (void)hipFuncSetAttribute((const void*)fused_kernel, hipFuncAttributeMaxDynamicSharedMemorySize, LDS_BYTES);
  fused_kernel<<<256, 256, LDS_BYTES, stream>>>(idx, table, W1, (char*)d_ws, xw, ww, bw, b1, W2, b2, d_out, d_ws);
}
```
